```python
import torch

def _softmax(x, axis=-1):
    return torch.softmax(x, dim=axis)
def _gelu(x):
    return torch.nn.functional.gelu(x, approximate="tanh")
def _silu(x):
    return torch.nn.functional.silu(x)
def _sigmoid(x):
    return torch.sigmoid(x)
def _layernorm(x, axis=-1, eps=1e-5):
    m = x.mean(dim=axis, keepdim=True)
    v = x.var(dim=axis, keepdim=True, unbiased=False)
    return (x - m) / torch.sqrt(v + eps)
def _scatter_add(tgt, idx, src, axis=0):
    return tgt.clone().index_add_(axis, idx, src)
def _segment_sum(src, seg, n):
    out = torch.zeros((n,) + src.shape[1:], dtype=src.dtype)
    return out.index_add_(0, seg, src)
def _pad(x, pw):
    flat = tuple(v for pair in reversed(pw) for v in pair)
    return torch.nn.functional.pad(x, flat)

kernel_name = 'sequential_preLN__sliding_block__glu_gated_reglu__prompt'

def setup_inputs():

    _w = (lambda a: (lambda t: t.to(torch.bfloat16) if t.is_floating_point() else t)(torch.as_tensor(a)))
    def _A_setup():
        B, S, D = 1, 2048, 512
        return {
            "x": _w((lambda *s: torch.randn(*s, dtype=torch.bfloat16) * 0.05)(B, S, D)),
            "wqkv": _w((lambda *s: torch.randn(*s, dtype=torch.bfloat16) * 0.05)(D, 3 * D)),
            "wo": _w((lambda *s: torch.randn(*s, dtype=torch.bfloat16) * 0.05)(D, D)),
        }
    _A_in = _A_setup()

    def _B_setup():
        B, S, D, F = 1, 2048, 512, 1792
        return {
            "x": _w((lambda *s: torch.randn(*s, dtype=torch.bfloat16) * 0.05)(B, S, D)),
            "W_gate": _w((lambda *s: torch.randn(*s, dtype=torch.bfloat16) * 0.8)(D, F) * (2.0 / D) ** 0.5),
            "W_up": _w((lambda *s: torch.randn(*s, dtype=torch.bfloat16) * 0.8)(D, F) * (2.0 / D) ** 0.5),
            "W_down": _w((lambda *s: torch.randn(*s, dtype=torch.bfloat16) * 0.8)(F, D) * (1.0 / F) ** 0.5),
        }
    _B_in = _B_setup()

    return {
        "A_x": _A_in["x"],
        "A_wqkv": _A_in["wqkv"],
        "A_wo": _A_in["wo"],
        "B_x": _B_in["x"],
        "B_W_gate": _B_in["W_gate"],
        "B_W_up": _B_in["W_up"],
        "B_W_down": _B_in["W_down"],
    }

def reference(A_x, A_wqkv, A_wo, B_x, B_W_gate, B_W_up, B_W_down):
    def _A_ref(x, wqkv, wo):
        B, S, D = x.shape
        H = 32; Dh = D // H; W = 256
        qkv = (lambda x, *d: x.permute(*d))((x @ wqkv).reshape(B, S, 3, H, Dh), 2, 0, 3, 1, 4)
        q, k, v = qkv[0], qkv[1], qkv[2]
        scores = torch.einsum("bhqd,bhkd->bhqk", q, k) * (Dh ** -0.5)
        idx = torch.arange(S)
        mask = (idx[:, None] - idx[None, :] >= 0) & (idx[:, None] - idx[None, :] < W)
        scores = torch.where(mask[None, None], scores, -1e9)
        p = _softmax(scores, axis=-1)
        o = torch.einsum("bhqk,bhkd->bhqd", p, v)
        return (lambda x, *d: x.permute(*d))(o, 0, 2, 1, 3).reshape(B, S, D) @ wo

    def _B_ref(x, W_gate, W_up, W_down):
        gate = torch.einsum("bsd,df->bsf", x, W_gate)
        up = torch.einsum("bsd,df->bsf", x, W_up)
        gate = gate * (lambda x, dt: x.to(dt))(gate > 0, gate.dtype)
        h = gate * up
        return torch.einsum("bsf,fd->bsd", h, W_down)

    x = A_x
    h = x + _A_ref(_layernorm(x, axis=-1), A_wqkv, A_wo)
    h2 = h + _B_ref(_layernorm(h, axis=-1), B_W_gate, B_W_up, B_W_down)
    return h2
```

```cpp
#include <hip/hip_runtime.h>
#include <hip/hip_bf16.h>
#include <stdint.h>

typedef __attribute__((ext_vector_type(8))) __bf16 bf16x8;
typedef __attribute__((ext_vector_type(2))) __bf16 bf16x2;
typedef __attribute__((ext_vector_type(16))) float f32x16;
typedef __attribute__((ext_vector_type(8))) float f32x8;
typedef __attribute__((ext_vector_type(4))) float f32x4;
typedef __attribute__((ext_vector_type(2))) float f32x2;
typedef __attribute__((ext_vector_type(4))) unsigned u32x4;
typedef __attribute__((ext_vector_type(2))) unsigned u32x2;
typedef __attribute__((ext_vector_type(4))) short s16x4;
typedef __attribute__((address_space(3))) s16x4* lds_s16x4;
typedef __attribute__((address_space(3))) void* lds_vp;
typedef const __attribute__((address_space(1))) void* g_vp;

#define DI __device__ __forceinline__
#define MFMA32(a, b, c) __builtin_amdgcn_mfma_f32_32x32x16_bf16((a), (b), (c), 0, 0, 0)
#define BARRIER() asm volatile("s_barrier" ::: "memory")
#define WAIT_VM(n) asm volatile("s_waitcnt vmcnt(" #n ")" ::: "memory")
#define WAIT_LGKM0() asm volatile("s_waitcnt lgkmcnt(0)" ::: "memory")
#define SCHED_FENCE() __builtin_amdgcn_sched_barrier(0)

constexpr int S = 2048, D = 512, F = 1792;
constexpr float LN_EPS = 1e-5f;
constexpr float Q_SCALE = 0.25f * 1.4426950408889634f;

constexpr size_t WS_Q = 0;
constexpr size_t WS_K = WS_Q + (2u << 20);
constexpr size_t WS_V = WS_K + (2u << 20);
constexpr size_t WS_O = WS_V + (2u << 20);
constexpr size_t WS_H = WS_O + (2u << 20);
constexpr size_t WS_ST = WS_H + (2u << 20);
constexpr size_t WS_M = WS_ST + (256u << 10);
constexpr size_t WS_WO = WS_M + (7u << 20);
constexpr size_t WS_WGU = WS_WO + (512u << 10);
constexpr size_t WS_WD = WS_WGU + (3584u << 10);

DI void glds16(const void* g, void* l) { __builtin_amdgcn_global_load_lds((g_vp)g, (lds_vp)l, 16, 0, 0); }
DI bf16x8 lds_frag(const char* p) { return *(const bf16x8*)p; }
DI bf16x8 tr_frag(const char* p0, const char* p1) {
  s16x4 lo = __builtin_amdgcn_ds_read_tr16_b64_v4i16((lds_s16x4)p0);
  s16x4 hi = __builtin_amdgcn_ds_read_tr16_b64_v4i16((lds_s16x4)p1);
  return __builtin_bit_cast(bf16x8, __builtin_shufflevector(lo, hi, 0, 1, 2, 3, 4, 5, 6, 7));
}
DI bf16x8 pack8(float a0, float a1, float a2, float a3, float a4, float a5, float a6, float a7) {
  f32x8 v = {a0, a1, a2, a3, a4, a5, a6, a7};
  return __builtin_convertvector(v, bf16x8);
}
DI float bf_lo(unsigned u) { return __uint_as_float(u << 16); }
DI float bf_hi(unsigned u) { return __uint_as_float(u & 0xffff0000u); }
DI float half_swap_sum(float v) {
  auto r = __builtin_amdgcn_permlane32_swap(__float_as_uint(v), __float_as_uint(v), false, false);
  return __uint_as_float(r[0]) + __uint_as_float(r[1]);
}
DI float half_swap_max(float v) {
  auto r = __builtin_amdgcn_permlane32_swap(__float_as_uint(v), __float_as_uint(v), false, false);
  return fmaxf(__uint_as_float(r[0]), __uint_as_float(r[1]));
}

constexpr int P1_SLOT = 28672, P1_NSLOT = 5, P1_AUX = P1_SLOT * P1_NSLOT;
constexpr int P1_LDS = P1_AUX + 4096;

template <int VMASK>
DI void p1_compute(char* smem, int w, int lane, int p, int j, char* ws) {
  const int r = lane & 31, h = lane >> 5;
  const int row = 32 * w + r, sw = (r >> 1) & 7;
  int xoff[4];
#pragma unroll
  for (int ks = 0; ks < 4; ++ks) xoff[ks] = row * 128 + (((2 * ks + h) ^ sw) << 4);
  const int q = (lane & 15) >> 2, pp = lane & 3, blk = (lane >> 4) & 1;
  const int woff = 16384 + (8 * h + q) * 192 + (2 * blk + (pp >> 1)) * 16 + 8 * (pp & 1);
  f32x16 acc[3] = {};
  float cs[3] = {0.f, 0.f, 0.f};
  const bf16x2 ones = {(__bf16)1.0f, (__bf16)1.0f};
  bf16x8 xf[2], wf[2][3];
  auto rd = [&](int buf, int s, int ks) {
    const char* sl = smem + (s % P1_NSLOT) * P1_SLOT;
    xf[buf] = lds_frag(sl + xoff[ks]);
#pragma unroll
    for (int c = 0; c < 3; ++c) wf[buf][c] = tr_frag(sl + woff + (16 * ks) * 192 + c * 64, sl + woff + (16 * ks + 4) * 192 + c * 64);
  };
  BARRIER();
  rd(0, 0, 0);
#pragma unroll
  for (int s = 0; s < 8; ++s) {
#pragma unroll
    for (int ks = 0; ks < 4; ++ks) {
      const int cur = ks & 1;
      if (ks < 3) rd(cur ^ 1, s, ks + 1);
      else if (s < 7) { WAIT_LGKM0(); BARRIER(); rd(cur ^ 1, s + 1, 0); }
#pragma unroll
      for (int c = 0; c < 3; ++c) {
        if ((VMASK >> c) & 1) acc[c] = MFMA32(xf[cur], wf[cur][c], acc[c]);
        else acc[c] = MFMA32(wf[cur][c], xf[cur], acc[c]);
      }
      if (ks == w) {
#pragma unroll
        for (int c = 0; c < 3; ++c) {
          u32x4 u = __builtin_bit_cast(u32x4, wf[cur][c]);
#pragma unroll
          for (int i = 0; i < 4; ++i) { const unsigned e = u[i]; cs[c] = __builtin_amdgcn_fdot2_f32_bf16(__builtin_bit_cast(bf16x2, e), ones, cs[c], false); }
        }
      }
    }
  }
  float* csp = (float*)(smem + P1_AUX);
  float* cst = csp + 384;
  f32x2* st = (f32x2*)(smem + P1_AUX + 2048);
#pragma unroll
  for (int c = 0; c < 3; ++c) { float t = half_swap_sum(cs[c]); if (h == 0) csp[w * 96 + c * 32 + r] = t; }
  WAIT_LGKM0(); BARRIER();
  if (w < 2) { int t = w * 64 + lane; if (t < 96) cst[t] = csp[t] + csp[96 + t] + csp[192 + t] + csp[288 + t]; }
  WAIT_LGKM0(); BARRIER();
  const int tb = 4 * p + w;
  const f32x2 mine = st[row];
#pragma unroll
  for (int c = 0; c < 3; ++c) {
    const int c0 = 96 * j + 32 * c;
    if ((VMASK >> c) & 1) {
      const float csv = cst[32 * c + r];
      float v[16];
#pragma unroll
      for (int i = 0; i < 16; ++i) { f32x2 t = st[32 * w + (i & 3) + 8 * (i >> 2) + 4 * h]; v[i] = acc[c][i] * t[0] - t[1] * csv; }
      char* dst = ws + WS_V + ((size_t)((c0 - 1024) >> 5) * 128 + 2 * tb) * 1024 + lane * 16;
      *(bf16x8*)dst = pack8(v[0], v[1], v[2], v[3], v[4], v[5], v[6], v[7]);
      *(bf16x8*)(dst + 1024) = pack8(v[8], v[9], v[10], v[11], v[12], v[13], v[14], v[15]);
    } else {
      const bool isq = c0 < 512;
      const float sc = isq ? Q_SCALE : 1.0f;
      const float a = mine[0] * sc, b = mine[1] * sc;
      float v[16];
#pragma unroll
      for (int g = 0; g < 4; ++g) {
        f32x4 cv = *(const f32x4*)(cst + 32 * c + 8 * g + 4 * h);
#pragma unroll
        for (int i = 0; i < 4; ++i) v[4 * g + i] = acc[c][4 * g + i] * a - b * cv[i];
      }
      const int head = (c0 & 511) >> 4;
      char* dst = ws + (isq ? WS_Q : WS_K) + ((size_t)head * 64 + tb) * 1024 + lane * 16;
      *(bf16x8*)dst = pack8(v[0], v[1], v[2], v[3], v[4], v[5], v[6], v[7]);
      *(bf16x8*)(dst + 65536) = pack8(v[8], v[9], v[10], v[11], v[12], v[13], v[14], v[15]);
    }
  }
}

DI void p1_helper(char* smem, int lw, int lane, int p, int j, const char* x, const char* wqkv) {
  unsigned xo[4], wo_[3];
#pragma unroll
  for (int i = 0; i < 4; ++i) {
    int row = 32 * lw + 8 * i + (lane >> 3);
    xo[i] = (128 * p + row) * 1024 + (((lane & 7) ^ ((row >> 1) & 7)) << 4);
  }
#pragma unroll
  for (int i = 0; i < 3; ++i) { int g = 64 * (3 * lw + i) + lane; int row = g / 12, c = g % 12; wo_[i] = row * 3072 + 192 * j + c * 16; }
  auto issue = [&](int s) {
    char* sl = smem + (s % P1_NSLOT) * P1_SLOT;
#pragma unroll
    for (int i = 0; i < 4; ++i) glds16(x + s * 128 + xo[i], sl + (4 * lw + i) * 1024);
#pragma unroll
    for (int i = 0; i < 3; ++i) glds16(wqkv + (size_t)s * 64 * 3072 + wo_[i], sl + 16384 + (3 * lw + i) * 1024);
  };
#pragma unroll
  for (int s = 0; s < 5; ++s) issue(s);
  const int r = lane & 31, h = lane >> 5, row = 32 * lw + r, sw = (r >> 1) & 7;
  f32x2 sum = {0.f, 0.f}, sq = {0.f, 0.f};
  auto stats = [&](int s) {
    const char* sl = smem + (s % P1_NSLOT) * P1_SLOT + row * 128;
#pragma unroll
    for (int c = 0; c < 4; ++c) {
      u32x4 u = *(const u32x4*)(sl + (((4 * h + c) ^ sw) << 4));
#pragma unroll
      for (int i = 0; i < 4; ++i) { f32x2 v = {bf_lo(u[i]), bf_hi(u[i])}; sum += v; sq += v * v; }
    }
  };
  WAIT_VM(28); BARRIER();
  stats(0); WAIT_VM(21); WAIT_LGKM0(); BARRIER();
  issue(5); stats(1); WAIT_VM(21); WAIT_LGKM0(); BARRIER();
  issue(6); stats(2); WAIT_VM(21); WAIT_LGKM0(); BARRIER();
  issue(7); stats(3); WAIT_VM(21); WAIT_LGKM0(); BARRIER();
  stats(4); WAIT_VM(14); WAIT_LGKM0(); BARRIER();
  stats(5); WAIT_VM(7); WAIT_LGKM0(); BARRIER();
  stats(6); WAIT_VM(0); WAIT_LGKM0(); BARRIER();
  stats(7);
  const float s1 = half_swap_sum(sum[0] + sum[1]), s2 = half_swap_sum(sq[0] + sq[1]);
  const float mean = s1 * (1.0f / D);
  const float var = fmaxf(s2 * (1.0f / D) - mean * mean, 0.f);
  const float rstd = 1.0f / sqrtf(var + LN_EPS);
  f32x2* st = (f32x2*)(smem + P1_AUX + 2048);
  if (h == 0) st[row] = f32x2{rstd, mean * rstd};
  WAIT_LGKM0(); BARRIER();
  BARRIER();
}

__global__ __launch_bounds__(512, 2) void k_qkv(const char* __restrict__ x, const char* __restrict__ wqkv, char* __restrict__ ws) {
  extern __shared__ __attribute__((aligned(16))) char smem[];
  const int w = __builtin_amdgcn_readfirstlane(threadIdx.x >> 6), lane = threadIdx.x & 63;
  const int xcd = blockIdx.x & 7, i = blockIdx.x >> 3;
  const int p = 4 * (xcd & 3) + (i & 3), j = 8 * (xcd >> 2) + (i >> 2);
  if (w >= 4) p1_helper(smem, w - 4, lane, p, j, x, wqkv);
  else if (j <= 9) p1_compute<0>(smem, w, lane, p, j, ws);
  else if (j == 10) p1_compute<4>(smem, w, lane, p, j, ws);
  else p1_compute<7>(smem, w, lane, p, j, ws);
}

constexpr int P2_K = 0, P2_V = 24576, P2_VP = 1040, P2_Q = P2_V + 24 * P2_VP, P2_PREP = P2_Q + 8192;
constexpr int P2_LDS = P2_PREP + 8 * 4096;

template <bool FULL>
DI void attn_task(const char* smem, int lane, int hh, int wq, int qb, char* ws, int head) {
  const int r = lane & 31, h = lane >> 5;
  const char* kb_ = smem + P2_K + (hh * 12 + wq) * 1024 + lane * 16;
  const bool valid = (r >> 4) == hh;
  const char* vb_ = smem + P2_V + 2 * wq * P2_VP + (valid ? lane * 16 : 1024);
  const bf16x8 qf = lds_frag(smem + P2_Q + (hh * 4 + wq) * 1024 + lane * 16);
  const f32x16 zero = {};
  const int t0 = FULL ? 1 : ((8 - qb) > 1 ? (8 - qb) : 1);
  const bool have_first = FULL || qb >= 8;
  f32x16 sm;
  {
    const f32x16 sd = MFMA32(lds_frag(kb_ + 8 * 1024), qf, zero);
    f32x16 sf;
    if (have_first) sf = MFMA32(lds_frag(kb_), qf, zero);
    else { for (int i = 0; i < 16; ++i) sf[i] = -1e30f; }
#pragma unroll
    for (int i = 0; i < 16; ++i) { const int jj = (i & 3) + 8 * (i >> 2) + 4 * h; sm[i] = jj <= r ? sd[i] : sf[i]; }
  }
  float m = sm[0];
#pragma unroll
  for (int i = 1; i < 16; ++i) m = fmaxf(m, sm[i]);
  if (FULL) {
#pragma unroll
    for (int t = 1; t < 8; ++t) {
      const f32x16 s = MFMA32(lds_frag(kb_ + t * 1024), qf, zero);
#pragma unroll
      for (int i = 0; i < 16; i += 2) m = fmaxf(fmaxf(m, s[i]), s[i + 1]);
    }
  } else {
    for (int t = t0; t < 8; ++t) {
      const f32x16 s = MFMA32(lds_frag(kb_ + t * 1024), qf, zero);
#pragma unroll
      for (int i = 0; i < 16; i += 2) m = fmaxf(fmaxf(m, s[i]), s[i + 1]);
    }
  }
  m = half_swap_max(m);
  f32x16 negm;
#pragma unroll
  for (int i = 0; i < 16; ++i) negm[i] = -m;
  f32x16 o = zero;
  {
    float pv[16];
#pragma unroll
    for (int i = 0; i < 16; ++i) pv[i] = __builtin_amdgcn_exp2f(sm[i] - m);
    u32x4 pa = __builtin_bit_cast(u32x4, pack8(pv[0], pv[1], pv[2], pv[3], pv[4], pv[5], pv[6], pv[7]));
    u32x4 pb = __builtin_bit_cast(u32x4, pack8(pv[8], pv[9], pv[10], pv[11], pv[12], pv[13], pv[14], pv[15]));
    u32x4 da, db;
#pragma unroll
    for (int i = 0; i < 4; ++i) {
      const int ja = ((2 * i) & 3) + 8 * ((2 * i) >> 2) + 4 * h;
      const unsigned ma = (ja <= r ? 0xffffu : 0u) | (ja + 1 <= r ? 0xffff0000u : 0u);
      const unsigned mb = (ja + 16 <= r ? 0xffffu : 0u) | (ja + 17 <= r ? 0xffff0000u : 0u);
      da[i] = pa[i] & ma; db[i] = pb[i] & mb;
    }
    o = MFMA32(lds_frag(vb_ + 16 * P2_VP), __builtin_bit_cast(bf16x8, da), o);
    o = MFMA32(lds_frag(vb_ + 17 * P2_VP), __builtin_bit_cast(bf16x8, db), o);
    if (have_first) {
      o = MFMA32(lds_frag(vb_), __builtin_bit_cast(bf16x8, pa ^ da), o);
      o = MFMA32(lds_frag(vb_ + P2_VP), __builtin_bit_cast(bf16x8, pb ^ db), o);
    }
  }
  auto tile = [&](int t) {
    const f32x16 s = MFMA32(lds_frag(kb_ + t * 1024), qf, negm);
    float pv[16];
#pragma unroll
    for (int i = 0; i < 16; ++i) pv[i] = __builtin_amdgcn_exp2f(s[i]);
    o = MFMA32(lds_frag(vb_ + (2 * t) * P2_VP), pack8(pv[0], pv[1], pv[2], pv[3], pv[4], pv[5], pv[6], pv[7]), o);
    o = MFMA32(lds_frag(vb_ + (2 * t + 1) * P2_VP), pack8(pv[8], pv[9], pv[10], pv[11], pv[12], pv[13], pv[14], pv[15]), o);
  };
  if (FULL) {
#pragma unroll
    for (int t = 1; t < 8; ++t) tile(t);
  } else {
    for (int t = t0; t < 8; ++t) tile(t);
  }
  const float l = hh ? o[0] : o[8];
  const float inv = 1.0f / l;
  float ov[8];
#pragma unroll
  for (int i = 0; i < 8; ++i) ov[i] = (hh ? o[8 + i] : o[i]) * inv;
  char* dst = ws + WS_O + (((size_t)(qb >> 1) * 32 + head) * 2 + (qb & 1)) * 1024 + lane * 16;
  *(bf16x8*)dst = pack8(ov[0], ov[1], ov[2], ov[3], ov[4], ov[5], ov[6], ov[7]);
}

DI void prep_issue(char* st, int u, int lane, const char* wo, const char* wg, const char* wu, const char* wd) {
  const int rr = lane >> 3, ch = (lane & 7) * 16;
  if (u < 576) {
    const bool iso = u < 128;
    const int v = iso ? u : u - 128;
    const int c = v & 7, kp = v >> 3;
    const char* src = (iso ? wo : wd) + (size_t)(32 * kp + rr) * 1024 + c * 128 + ch;
#pragma unroll
    for (int i = 0; i < 4; ++i) glds16(src + i * 8 * 1024, st + i * 1024);
  } else {
    const int v = u - 576, cg = v % 28, ks = v / 28;
    const size_t off = (size_t)(16 * ks + rr) * 3584 + cg * 128 + ch;
    glds16(wg + off, st); glds16(wg + off + 8 * 3584, st + 1024);
    glds16(wu + off, st + 2048); glds16(wu + off + 8 * 3584, st + 3072);
  }
}
DI void prep_finish(const char* st, int u, int lane, char* ws) {
  const int h = lane >> 5, blk = (lane >> 4) & 1, q = (lane & 15) >> 2, pp = lane & 3;
  if (u < 576) {
    const bool iso = u < 128;
    const int v = iso ? u : u - 128;
    const int c = v & 7, kp = v >> 3;
    char* dst = ws + (iso ? WS_WO : WS_WD) + ((size_t)c * (iso ? 32 : 112) + 2 * kp) * 2048 + lane * 16;
    const char* b = st + (4 * h + q) * 128 + (2 * blk + (pp >> 1)) * 16 + 8 * (pp & 1);
#pragma unroll
    for (int k2 = 0; k2 < 2; ++k2)
#pragma unroll
      for (int nt = 0; nt < 2; ++nt)
        *(bf16x8*)(dst + k2 * 2048 + nt * 1024) = tr_frag(b + k2 * 16 * 128 + nt * 64, b + (k2 * 16 + 8) * 128 + nt * 64);
  } else {
    const int v = u - 576, cg = v % 28, ks = v / 28;
    const char* b = st + blk * 2048 + (4 * h + q) * 128 + (pp >> 1) * 16 + 8 * (pp & 1);
#pragma unroll
    for (int i = 0; i < 4; ++i) {
      const int fs = 4 * cg + i, j = fs / 7, t = fs % 7;
      *(bf16x8*)(ws + WS_WGU + (((size_t)j * 32 + ks) * 7 + t) * 1024 + lane * 16) = tr_frag(b + i * 32, b + 8 * 128 + i * 32);
    }
  }
}

__global__ __launch_bounds__(512, 2) void k_attn(char* __restrict__ ws, const char* __restrict__ wo, const char* __restrict__ wg,
                                                 const char* __restrict__ wu, const char* __restrict__ wd) {
  extern __shared__ __attribute__((aligned(16))) char smem[];
  const int w = __builtin_amdgcn_readfirstlane(threadIdx.x >> 6), lane = threadIdx.x & 63;
  const int xcd = blockIdx.x & 7, i = blockIdx.x >> 3;
  const int hp = 2 * xcd + (i & 1), p = i >> 1;
  const int kb0 = 4 * p - 8;
  const int hh = w >> 2, wq = w & 3, qb = 4 * p + wq, head = 2 * hp + hh;
  glds16(ws + WS_Q + ((size_t)head * 64 + qb) * 1024 + lane * 16, smem + P2_Q + w * 1024);
#pragma unroll
  for (int n = 0; n < 3; ++n) {
    const int t = wq * 3 + n;
    if (kb0 + t >= 0) glds16(ws + WS_K + ((size_t)head * 64 + kb0 + t) * 1024 + lane * 16, smem + P2_K + (hh * 12 + t) * 1024);
    const int sv = w * 3 + n;
    if (2 * kb0 + sv >= 0) glds16(ws + WS_V + ((size_t)hp * 128 + 2 * kb0 + sv) * 1024 + lane * 16, smem + P2_V + sv * P2_VP);
  }
  if (threadIdx.x < 24) *(u32x4*)(smem + P2_V + threadIdx.x * P2_VP + 1024) = u32x4{0x3f803f80u, 0x3f803f80u, 0x3f803f80u, 0x3f803f80u};
  WAIT_VM(0); WAIT_LGKM0(); BARRIER();
  const int u = blockIdx.x + 256 * w;
  char* stg = smem + P2_PREP + w * 4096;
  if (u < 1472) prep_issue(stg, u, lane, wo, wg, wu, wd);
  if (qb >= 8) attn_task<true>(smem, lane, hh, wq, qb, ws, head);
  else attn_task<false>(smem, lane, hh, wq, qb, ws, head);
  if (u < 1472) { WAIT_VM(0); prep_finish(stg, u, lane, ws); }
}

template <int KS, int DEPTH, bool LAST>
DI void proj_phase(char* smem, const char* xf_g, const char* wf_g, char* ws, const char* xin, char* out, int tp, int c) {
  const int w = __builtin_amdgcn_readfirstlane(threadIdx.x >> 6), lane = threadIdx.x & 63;
  const int r = lane & 31, h = lane >> 5;
  const int tt = w >> 1, nt = w & 1, tb = 2 * tp + tt, ntile = 2 * c + nt;
  const char* xp = xf_g + ((size_t)tp * KS * 4 + KS * w) * 2048 + lane * 16;
  const char* wp = wf_g + ((size_t)c * KS * 4 + KS * w) * 2048 + lane * 16;
  u32x2 rx[4]; u32x4 rh[2];
  if (!LAST) {
#pragma unroll
    for (int g = 0; g < 4; ++g) rx[g] = *(const u32x2*)(xin + (size_t)(32 * tb + r) * 1024 + (32 * ntile + 8 * g + 4 * h) * 2);
  } else {
#pragma unroll
    for (int s = 0; s < 2; ++s) rh[s] = *(const u32x4*)(ws + WS_H + ((size_t)tb * 32 + 2 * ntile + s) * 1024 + lane * 16);
  }
  bf16x8 xf[KS][2], wf[KS][2];
  f32x16 acc[4] = {};
#pragma unroll
  for (int k = 0; k < DEPTH; ++k) {
    xf[k][0] = *(const bf16x8*)(xp + k * 2048); wf[k][0] = *(const bf16x8*)(wp + k * 2048);
    xf[k][1] = *(const bf16x8*)(xp + k * 2048 + 1024); wf[k][1] = *(const bf16x8*)(wp + k * 2048 + 1024);
  }
  SCHED_FENCE();
#pragma unroll
  for (int k = 0; k < KS; ++k) {
    if (k + DEPTH < KS) {
      const int n = k + DEPTH;
      xf[n][0] = *(const bf16x8*)(xp + n * 2048); wf[n][0] = *(const bf16x8*)(wp + n * 2048);
      xf[n][1] = *(const bf16x8*)(xp + n * 2048 + 1024); wf[n][1] = *(const bf16x8*)(wp + n * 2048 + 1024);
    }
    acc[0] = MFMA32(wf[k][0], xf[k][0], acc[0]);
    acc[1] = MFMA32(wf[k][1], xf[k][0], acc[1]);
    acc[2] = MFMA32(wf[k][0], xf[k][1], acc[2]);
    acc[3] = MFMA32(wf[k][1], xf[k][1], acc[3]);
    SCHED_FENCE();
  }
#pragma unroll
  for (int t = 0; t < 4; ++t) {
    if (t != w) {
#pragma unroll
      for (int g = 0; g < 4; ++g)
        *(f32x4*)(smem + ((w * 4 + t) * 4 + g) * 1024 + lane * 16) = f32x4{acc[t][4 * g], acc[t][4 * g + 1], acc[t][4 * g + 2], acc[t][4 * g + 3]};
    }
  }
  __syncthreads();
  f32x16 a = w == 0 ? acc[0] : w == 1 ? acc[1] : w == 2 ? acc[2] : acc[3];
#pragma unroll
  for (int o = 1; o < 4; ++o) {
    const int sw = (w + o) & 3;
#pragma unroll
    for (int g = 0; g < 4; ++g) {
      const f32x4 v = *(const f32x4*)(smem + ((sw * 4 + w) * 4 + g) * 1024 + lane * 16);
      a[4 * g] += v[0]; a[4 * g + 1] += v[1]; a[4 * g + 2] += v[2]; a[4 * g + 3] += v[3];
    }
  }
  if (!LAST) {
#pragma unroll
    for (int g = 0; g < 4; ++g) {
      a[4 * g] += bf_lo(rx[g][0]); a[4 * g + 1] += bf_hi(rx[g][0]); a[4 * g + 2] += bf_lo(rx[g][1]); a[4 * g + 3] += bf_hi(rx[g][1]);
    }
    float s1 = 0.f, s2 = 0.f;
#pragma unroll
    for (int s = 0; s < 2; ++s) {
      const u32x4 u = __builtin_bit_cast(u32x4, pack8(a[8 * s], a[8 * s + 1], a[8 * s + 2], a[8 * s + 3], a[8 * s + 4], a[8 * s + 5], a[8 * s + 6], a[8 * s + 7]));
      *(u32x4*)(ws + WS_H + ((size_t)tb * 32 + 2 * ntile + s) * 1024 + lane * 16) = u;
#pragma unroll
      for (int i = 0; i < 4; ++i) { const float lo = bf_lo(u[i]), hi = bf_hi(u[i]); s1 += lo + hi; s2 += lo * lo + hi * hi; }
    }
    s1 = half_swap_sum(s1); s2 = half_swap_sum(s2);
    if (h == 0) *(f32x2*)(ws + WS_ST + (((size_t)tb * 16 + ntile) * 32 + r) * 8) = f32x2{s1, s2};
  } else {
    unsigned pk[8];
#pragma unroll
    for (int s = 0; s < 2; ++s) {
#pragma unroll
      for (int i = 0; i < 4; ++i) { a[8 * s + 2 * i] += bf_lo(rh[s][i]); a[8 * s + 2 * i + 1] += bf_hi(rh[s][i]); }
      const u32x4 u = __builtin_bit_cast(u32x4, pack8(a[8 * s], a[8 * s + 1], a[8 * s + 2], a[8 * s + 3], a[8 * s + 4], a[8 * s + 5], a[8 * s + 6], a[8 * s + 7]));
#pragma unroll
      for (int i = 0; i < 4; ++i) pk[4 * s + i] = u[i];
    }
    char* dst = out + (size_t)(32 * tb + r) * 1024 + (32 * ntile + 8 * h) * 2;
#pragma unroll
    for (int s = 0; s < 2; ++s) {
      auto a0 = __builtin_amdgcn_permlane32_swap(pk[4 * s], pk[4 * s + 2], false, false);
      auto a1 = __builtin_amdgcn_permlane32_swap(pk[4 * s + 1], pk[4 * s + 3], false, false);
      *(u32x4*)(dst + s * 32) = u32x4{a0[0], a1[0], a0[1], a1[1]};
    }
  }
}

__global__ __launch_bounds__(256, 1) void k_oproj(char* __restrict__ ws, const char* __restrict__ x) {
  extern __shared__ __attribute__((aligned(16))) char smem[];
  const int xcd = blockIdx.x & 7, i = blockIdx.x >> 3;
  proj_phase<8, 8, false>(smem, ws + WS_O, ws + WS_WO, ws, x, nullptr, 4 * xcd + (i & 3), i >> 2);
}
__global__ __launch_bounds__(256, 1) void k_down(char* __restrict__ ws, char* __restrict__ out) {
  extern __shared__ __attribute__((aligned(16))) char smem[];
  const int xcd = blockIdx.x & 7, i = blockIdx.x >> 3;
  proj_phase<28, 12, true>(smem, ws + WS_M, ws + WS_WD, ws, nullptr, out, 4 * xcd + (i & 3), i >> 2);
}

constexpr int P4_SLOT = 45056, P4_LDS = 3 * P4_SLOT;

DI void p4_compute(char* smem, int w, int lane, int p, int j, char* ws) {
  const int r = lane & 31, h = lane >> 5, tb = 4 * p + w;
  float s1 = 0.f, s2 = 0.f;
#pragma unroll
  for (int i = 0; i < 8; ++i) { const f32x2 v = *(const f32x2*)(ws + WS_ST + (((size_t)tb * 16 + 8 * h + i) * 32 + r) * 8); s1 += v[0]; s2 += v[1]; }
  s1 = half_swap_sum(s1); s2 = half_swap_sum(s2);
  const float mean = s1 * (1.0f / D);
  const float rstd = 1.0f / sqrtf(fmaxf(s2 * (1.0f / D) - mean * mean, 0.f) + LN_EPS);
  const float mr = mean * rstd;
  f32x16 acc[7] = {};
  u32x4 xr[2]; bf16x8 wf[2][7];
  auto rd = [&](int buf, int s, int ks) {
    const char* sl = smem + (s % 3) * P4_SLOT + lane * 16;
    xr[buf] = *(const u32x4*)(sl + (w * 4 + ks) * 1024);
#pragma unroll
    for (int t = 0; t < 7; ++t) wf[buf][t] = lds_frag(sl + 16384 + (ks * 7 + t) * 1024);
  };
  BARRIER();
  rd(0, 0, 0);
#pragma unroll
  for (int s = 0; s < 8; ++s) {
#pragma unroll
    for (int ks = 0; ks < 4; ++ks) {
      const int cur = ks & 1;
      const u32x4 u = xr[cur];
      const bf16x8 xn = pack8(bf_lo(u[0]) * rstd - mr, bf_hi(u[0]) * rstd - mr, bf_lo(u[1]) * rstd - mr, bf_hi(u[1]) * rstd - mr,
                              bf_lo(u[2]) * rstd - mr, bf_hi(u[2]) * rstd - mr, bf_lo(u[3]) * rstd - mr, bf_hi(u[3]) * rstd - mr);
      if (ks < 3) rd(cur ^ 1, s, ks + 1);
      else if (s < 7) { WAIT_LGKM0(); BARRIER(); rd(cur ^ 1, s + 1, 0); }
#pragma unroll
      for (int t = 0; t < 7; ++t) acc[t] = MFMA32(wf[cur][t], xn, acc[t]);
    }
  }
  char* dst = ws + WS_M + (((size_t)(tb >> 1) * 112 + 7 * j) * 2 + (tb & 1)) * 1024 + lane * 16;
#pragma unroll
  for (int t = 0; t < 7; ++t) {
    float v[8];
#pragma unroll
    for (int i = 0; i < 8; ++i) v[i] = fmaxf(acc[t][i], 0.f) * acc[t][8 + i];
    *(bf16x8*)(dst + t * 2048) = pack8(v[0], v[1], v[2], v[3], v[4], v[5], v[6], v[7]);
  }
}

DI void p4_loader(char* smem, int lw, int lane, int p, int j, const char* ws) {
  const char* xs = ws + WS_H + (size_t)(4 * p + lw) * 32 * 1024 + lane * 16;
  const char* wsrc = ws + WS_WGU + ((size_t)j * 32 + lw) * 7 * 1024 + lane * 16;
  auto issue = [&](int s) {
    char* sl = smem + (s % 3) * P4_SLOT;
#pragma unroll
    for (int i = 0; i < 4; ++i) glds16(xs + (s * 4 + i) * 1024, sl + (lw * 4 + i) * 1024);
#pragma unroll
    for (int i = 0; i < 7; ++i) glds16(wsrc + (s * 28 + i) * 1024, sl + 16384 + (lw * 7 + i) * 1024);
  };
  issue(0); issue(1); issue(2);
  WAIT_VM(22); BARRIER();
  WAIT_VM(11); BARRIER();
  issue(3); WAIT_VM(11); BARRIER();
  issue(4); WAIT_VM(11); BARRIER();
  issue(5); WAIT_VM(11); BARRIER();
  issue(6); WAIT_VM(11); BARRIER();
  issue(7); WAIT_VM(11); BARRIER();
  WAIT_VM(0); BARRIER();
}

__global__ __launch_bounds__(512, 2) void k_gateup(char* __restrict__ ws) {
  extern __shared__ __attribute__((aligned(16))) char smem[];
  const int w = __builtin_amdgcn_readfirstlane(threadIdx.x >> 6), lane = threadIdx.x & 63;
  const int xcd = blockIdx.x & 7, i = blockIdx.x >> 3;
  const int p = 8 * (xcd & 1) + (i & 7), j = 4 * (xcd >> 1) + (i >> 3);
  if (w >= 4) p4_loader(smem, w - 4, lane, p, j, ws);
  else p4_compute(smem, w, lane, p, j, ws);
}

extern "C" void kernel_launch(void* const* d_in, const int* in_sizes, int n_in, void* d_out, int out_size, void* d_ws, size_t ws_size,
                              hipStream_t stream) {
  const char* x = (const char*)d_in[0];
  const char* wqkv = (const char*)d_in[1];
  const char* wo = (const char*)d_in[2];
  const char* wg = (const char*)d_in[4];
  const char* wu = (const char*)d_in[5];
  const char* wd = (const char*)d_in[6];
  char* ws = (char*)d_ws;
  (void)hipFuncSetAttribute((const void*)k_qkv, hipFuncAttributeMaxDynamicSharedMemorySize, P1_LDS);
  (void)hipFuncSetAttribute((const void*)k_attn, hipFuncAttributeMaxDynamicSharedMemorySize, P2_LDS);
  (void)hipFuncSetAttribute((const void*)k_gateup, hipFuncAttributeMaxDynamicSharedMemorySize, P4_LDS);
  k_qkv<<<256, 512, P1_LDS, stream>>>(x, wqkv, ws);
  k_attn<<<256, 512, P2_LDS, stream>>>(ws, wo, wg, wu, wd);
  k_oproj<<<256, 256, 65536, stream>>>(ws, x);
  k_gateup<<<256, 512, P4_LDS, stream>>>(ws);
  k_down<<<256, 256, 65536, stream>>>(ws, (char*)d_out);
}
```

```cpp
#include <hip/hip_runtime.h>
#include <hip/hip_bf16.h>
#include <stdint.h>

typedef __attribute__((ext_vector_type(8))) __bf16 bf16x8;
typedef __attribute__((ext_vector_type(2))) __bf16 bf16x2;
typedef __attribute__((ext_vector_type(16))) float f32x16;
typedef __attribute__((ext_vector_type(8))) float f32x8;
typedef __attribute__((ext_vector_type(4))) float f32x4;
typedef __attribute__((ext_vector_type(2))) float f32x2;
typedef __attribute__((ext_vector_type(4))) unsigned u32x4;
typedef __attribute__((ext_vector_type(2))) unsigned u32x2;
typedef __attribute__((ext_vector_type(4))) short s16x4;
typedef __attribute__((address_space(3))) s16x4* lds_s16x4;
typedef __attribute__((address_space(3))) void* lds_vp;
typedef const __attribute__((address_space(1))) void* g_vp;

#define DI __device__ __forceinline__
#define MFMA32(a, b, c) __builtin_amdgcn_mfma_f32_32x32x16_bf16((a), (b), (c), 0, 0, 0)
#define BARRIER() asm volatile("s_barrier" ::: "memory")
#define WAIT_VM(n) asm volatile("s_waitcnt vmcnt(" #n ")" ::: "memory")
#define WAIT_LGKM0() asm volatile("s_waitcnt lgkmcnt(0)" ::: "memory")
#define SCHED_FENCE() __builtin_amdgcn_sched_barrier(0)

constexpr int S = 2048, D = 512, F = 1792;
constexpr float LN_EPS = 1e-5f;
constexpr float Q_SCALE = 0.25f * 1.4426950408889634f;

constexpr unsigned WS_Q = 0;
constexpr unsigned WS_K = WS_Q + (2u << 20);
constexpr unsigned WS_V = WS_K + (2u << 20);
constexpr unsigned WS_O = WS_V + (2u << 20);
constexpr unsigned WS_H = WS_O + (2u << 20);
constexpr unsigned WS_ST = WS_H + (2u << 20);
constexpr unsigned WS_M = WS_ST + (256u << 10);
constexpr unsigned WS_WGU = WS_M + (7u << 20);
constexpr unsigned WS_CS = WS_WGU + (3584u << 10);
constexpr unsigned WS_CNT = WS_CS + (448u << 10);
constexpr unsigned WS_BYTES = WS_CNT + 48 * 128;

typedef __amdgpu_buffer_rsrc_t rsrc_t;
typedef __attribute__((address_space(3))) const bf16x8* lds_fp;
typedef __attribute__((address_space(1))) unsigned gu32;
constexpr int SC1 = 16;

#define PROF_ARG
#define PROF_PASS
#define PROF_BEGIN
#define STAMP(i)
#define PROF_END(kid, wsp)

DI rsrc_t make_rsrc(const void* p, unsigned bytes) { return __builtin_amdgcn_make_buffer_rsrc((void*)p, 0, bytes, 0x00020000); }
DI void glds16(const void* g, void* l) { __builtin_amdgcn_global_load_lds((g_vp)g, (lds_vp)l, 16, 0, 0); }
DI void blds16(rsrc_t rs, unsigned voff, unsigned soff, char* l) { __builtin_amdgcn_raw_ptr_buffer_load_lds(rs, (lds_vp)l, 16, voff, soff, 0, 0); }
DI u32x4 ld16_dev(rsrc_t rs, unsigned voff, unsigned soff) { return __builtin_amdgcn_raw_buffer_load_b128(rs, voff, soff, SC1); }
DI void st16_dev(rsrc_t rs, unsigned voff, unsigned soff, u32x4 v) { __builtin_amdgcn_raw_buffer_store_b128(v, rs, voff + soff, 0, SC1); }
#define P1_AUXBITS 16
DI void st16_p1(rsrc_t rs, unsigned voff, unsigned soff, u32x4 v) { __builtin_amdgcn_raw_buffer_store_b128(v, rs, voff + soff, 0, P1_AUXBITS); }
DI bf16x8 lds_frag(const char* p) { return *(const bf16x8*)p; }
DI bf16x8 lds_frag_a(unsigned a) { return *(lds_fp)(size_t)a; }
DI unsigned lds_addr(const char* p) { return (unsigned)(size_t)(const __attribute__((address_space(3))) char*)p; }
DI unsigned opaque(unsigned v) { asm volatile("" : "+v"(v)); return v; }
DI bf16x8 tr_frag(const char* p0, const char* p1) {
  s16x4 lo = __builtin_amdgcn_ds_read_tr16_b64_v4i16((lds_s16x4)p0);
  s16x4 hi = __builtin_amdgcn_ds_read_tr16_b64_v4i16((lds_s16x4)p1);
  return __builtin_bit_cast(bf16x8, __builtin_shufflevector(lo, hi, 0, 1, 2, 3, 4, 5, 6, 7));
}
DI bf16x8 tr_frag_a(unsigned a0, unsigned a1) {
  s16x4 lo = __builtin_amdgcn_ds_read_tr16_b64_v4i16((lds_s16x4)(size_t)a0);
  s16x4 hi = __builtin_amdgcn_ds_read_tr16_b64_v4i16((lds_s16x4)(size_t)a1);
  return __builtin_bit_cast(bf16x8, __builtin_shufflevector(lo, hi, 0, 1, 2, 3, 4, 5, 6, 7));
}
DI bf16x8 pack8(float a0, float a1, float a2, float a3, float a4, float a5, float a6, float a7) {
  f32x8 v = {a0, a1, a2, a3, a4, a5, a6, a7};
  return __builtin_convertvector(v, bf16x8);
}
DI u32x4 as_u4(bf16x8 v) { return __builtin_bit_cast(u32x4, v); }
DI bf16x8 as_frag(u32x4 v) { return __builtin_bit_cast(bf16x8, v); }
DI float bf_lo(unsigned u) { return __uint_as_float(u << 16); }
DI float bf_hi(unsigned u) { return __uint_as_float(u & 0xffff0000u); }
DI float half_swap_sum(float v) {
  auto r = __builtin_amdgcn_permlane32_swap(__float_as_uint(v), __float_as_uint(v), false, false);
  return __uint_as_float(r[0]) + __uint_as_float(r[1]);
}
DI float half_swap_max(float v) {
  auto r = __builtin_amdgcn_permlane32_swap(__float_as_uint(v), __float_as_uint(v), false, false);
  return fmaxf(__uint_as_float(r[0]), __uint_as_float(r[1]));
}
DI void add_f32(float& a, float v) { asm("v_add_f32 %0, %0, %1" : "+v"(a) : "v"(v)); }
DI void fma_sq(float& a, float v) { asm("v_fma_f32 %0, %1, %1, %0" : "+v"(a) : "v"(v)); }
DI float colsum_frag(bf16x8 f, float acc) {
  const bf16x2 ones = {(__bf16)1.0f, (__bf16)1.0f};
  const u32x4 u = as_u4(f);
#pragma unroll
  for (int i = 0; i < 4; ++i) { const unsigned e = u[i]; acc = __builtin_amdgcn_fdot2_f32_bf16(__builtin_bit_cast(bf16x2, e), ones, acc, false); }
  return acc;
}

DI void signal_panel(char* ws, int phase, int panel) {
  __hip_atomic_fetch_add((gu32*)(ws + WS_CNT + (phase * 16 + panel) * 128), 1u, __ATOMIC_RELAXED, __HIP_MEMORY_SCOPE_AGENT);
}
DI void wait_panel(char* ws, int phase, int panel) {
  gu32* word = (gu32*)(ws + WS_CNT + (phase * 16 + panel) * 128);
  for (unsigned spins = 0; spins < (1u << 19); ++spins) {
    if ((unsigned)__builtin_amdgcn_readfirstlane(__hip_atomic_load(word, __ATOMIC_RELAXED, __HIP_MEMORY_SCOPE_AGENT)) >= 16u) break;
  }
  asm volatile("" ::: "memory");
}
constexpr int P1_SLOT = 28672, P1_NSLOT = 5, P1_AUX = P1_SLOT * P1_NSLOT, P1_PREP = P1_AUX + 4096;
constexpr int P1_LDS = P1_PREP + 16384;

template <int VMASK>
DI void p1_compute(char* smem, int w, int lane, int p, int j, char* ws) {
  PROF_BEGIN
  const int r = lane & 31, h = lane >> 5;
  const int row = 32 * w + r, sw = (r >> 1) & 7;
  const unsigned lb = lds_addr(smem);
  unsigned xa[4][3], wa[3];
#pragma unroll
  for (int ks = 0; ks < 4; ++ks) {
    const unsigned o = lb + row * 128 + (((2 * ks + h) ^ sw) << 4);
#pragma unroll
    for (int b = 0; b < 3; ++b) xa[ks][b] = opaque(o + b * 2 * P1_SLOT);
  }
  {
    const int q = (lane & 15) >> 2, pp = lane & 3, blk = (lane >> 4) & 1;
    const unsigned o = lb + 16384 + (8 * h + q) * 192 + (2 * blk + (pp >> 1)) * 16 + 8 * (pp & 1);
#pragma unroll
    for (int b = 0; b < 3; ++b) wa[b] = opaque(o + b * 2 * P1_SLOT);
  }
  f32x16 acc[3] = {};
  float cs[3] = {0.f, 0.f, 0.f};
  bf16x8 xf[3], wf[3][3];
  auto rdx = [&](int g) { const int sl = (g >> 2) % P1_NSLOT; xf[g % 3] = lds_frag_a(xa[g & 3][sl >> 1] + (sl & 1) * P1_SLOT); };
  auto rdw = [&](int g, int c) {
    const int sl = (g >> 2) % P1_NSLOT;
    const unsigned a = wa[sl >> 1] + (sl & 1) * P1_SLOT + (16 * (g & 3)) * 192 + c * 64;
    wf[g % 3][c] = tr_frag_a(a, a + 4 * 192);
  };
  auto mm = [&](int g, int c) {
    if ((VMASK >> c) & 1) acc[c] = MFMA32(xf[g % 3], wf[g % 3][c], acc[c]);
    else acc[c] = MFMA32(wf[g % 3][c], xf[g % 3], acc[c]);
  };
  STAMP(1); BARRIER(); STAMP(2);
#pragma unroll
  for (int g = 0; g < 2; ++g) { rdx(g); rdw(g, 0); rdw(g, 1); rdw(g, 2); }
  SCHED_FENCE();
#pragma unroll
  for (int g = 0; g < 32; ++g) {
    const int n = g + 2;
    mm(g, 0); SCHED_FENCE();
    if (n < 32) {
      if ((n & 3) == 0) { WAIT_LGKM0(); BARRIER(); }
      rdx(n); rdw(n, 0); SCHED_FENCE();
    }
    mm(g, 1); SCHED_FENCE();
    if (n < 32) { rdw(n, 1); rdw(n, 2); SCHED_FENCE(); }
    mm(g, 2); SCHED_FENCE();
    if ((g & 3) == w) {
#pragma unroll
      for (int c = 0; c < 3; ++c) cs[c] = colsum_frag(wf[g % 3][c], cs[c]);
    }
    SCHED_FENCE();
  }
  STAMP(3);
  float* csp = (float*)(smem + P1_AUX);
  float* cst = csp + 384;
  f32x2* st = (f32x2*)(smem + P1_AUX + 2048);
#pragma unroll
  for (int c = 0; c < 3; ++c) { float t = half_swap_sum(cs[c]); if (h == 0) csp[w * 96 + c * 32 + r] = t; }
  WAIT_LGKM0(); BARRIER();
  if (w < 2) { int t = w * 64 + lane; if (t < 96) cst[t] = csp[t] + csp[96 + t] + csp[192 + t] + csp[288 + t]; }
  WAIT_LGKM0(); BARRIER();
  STAMP(4);
  const int tb = 4 * p + w;
  const rsrc_t rws = make_rsrc(ws, WS_BYTES);
  const f32x2 mine = st[row];
#pragma unroll
  for (int c = 0; c < 3; ++c) {
    const int c0 = 96 * j + 32 * c;
    if ((VMASK >> c) & 1) {
      const float csv = cst[32 * c + r];
      float v[16];
#pragma unroll
      for (int i = 0; i < 16; ++i) { f32x2 t = st[32 * w + (i & 3) + 8 * (i >> 2) + 4 * h]; v[i] = acc[c][i] * t[0] - t[1] * csv; }
      const unsigned dst = WS_V + (((c0 - 1024) >> 5) * 128 + 2 * tb) * 1024;
      st16_p1(rws, lane * 16, dst, as_u4(pack8(v[0], v[1], v[2], v[3], v[4], v[5], v[6], v[7])));
      st16_p1(rws, lane * 16, dst + 1024, as_u4(pack8(v[8], v[9], v[10], v[11], v[12], v[13], v[14], v[15])));
    } else {
      const bool isq = c0 < 512;
      const float sc = isq ? Q_SCALE : 1.0f;
      const float a = mine[0] * sc, b = mine[1] * sc;
      float v[16];
#pragma unroll
      for (int g = 0; g < 4; ++g) {
        f32x4 cv = *(const f32x4*)(cst + 32 * c + 8 * g + 4 * h);
#pragma unroll
        for (int i = 0; i < 4; ++i) v[4 * g + i] = acc[c][4 * g + i] * a - b * cv[i];
      }
      const int head = (c0 & 511) >> 4;
      const unsigned dst = (isq ? WS_Q : WS_K) + (head * 64 + tb) * 1024;
      st16_p1(rws, lane * 16, dst, as_u4(pack8(v[0], v[1], v[2], v[3], v[4], v[5], v[6], v[7])));
      st16_p1(rws, lane * 16, dst + 65536, as_u4(pack8(v[8], v[9], v[10], v[11], v[12], v[13], v[14], v[15])));
    }
  }
  STAMP(5); PROF_END(0, ws);
}

DI void p1_helper(char* smem, int lw, int lane, int p, int j, const char* x, const char* wqkv, const char* wg, const char* wu, char* ws) {
  PROF_BEGIN
  const rsrc_t rx = make_rsrc(x, S * D * 2), rw = make_rsrc(wqkv, D * 3 * D * 2);
  unsigned xo[4], wo_[3];
#pragma unroll
  for (int i = 0; i < 4; ++i) {
    int row = 32 * lw + 8 * i + (lane >> 3);
    xo[i] = (128 * p + row) * 1024 + (((lane & 7) ^ ((row >> 1) & 7)) << 4);
  }
#pragma unroll
  for (int i = 0; i < 3; ++i) { int g = 64 * (3 * lw + i) + lane; int row = g / 12, c = g % 12; wo_[i] = row * 3072 + 192 * j + c * 16; }
  auto issue = [&](int s) {
    char* sl = smem + (s % P1_NSLOT) * P1_SLOT;
#pragma unroll
    for (int i = 0; i < 4; ++i) blds16(rx, xo[i], s * 128, sl + (4 * lw + i) * 1024);
#pragma unroll
    for (int i = 0; i < 3; ++i) blds16(rw, wo_[i], s * 64 * 3072, sl + 16384 + (3 * lw + i) * 1024);
  };
  issue(0); issue(1); issue(2);
  STAMP(1);
  const int b = blockIdx.x, cg = b % 28, pks = 4 * (b / 28) + lw;
  const bool prep = b < 224;
  char* stg = smem + P1_PREP + lw * 4096;
  auto prep_issue = [&]() {
    const rsrc_t rg = make_rsrc(wg, D * F * 2), ru = make_rsrc(wu, D * F * 2);
    const unsigned off = (16 * pks + (lane >> 3)) * 3584 + cg * 128 + (lane & 7) * 16;
    blds16(rg, off, 0, stg); blds16(rg, off, 8 * 3584, stg + 1024);
    blds16(ru, off, 0, stg + 2048); blds16(ru, off, 8 * 3584, stg + 3072);
  };
  const int r = lane & 31, h = lane >> 5, row = 32 * lw + r, sw = (r >> 1) & 7;
  float sum0 = 0.f, sum1 = 0.f, sq0 = 0.f, sq1 = 0.f;
  auto piece = [&](int s, int i) {
    char* sl = smem + (s % P1_NSLOT) * P1_SLOT;
    if (i < 4) blds16(rx, xo[i], s * 128, sl + (4 * lw + i) * 1024);
    else blds16(rw, wo_[i - 4], s * 64 * 3072, sl + 16384 + (3 * lw + i - 4) * 1024);
  };
  auto step = [&](int s, int n) {
    const char* sl = smem + (s % P1_NSLOT) * P1_SLOT + row * 128;
    u32x4 u[4];
#pragma unroll
    for (int c = 0; c < 4; ++c) u[c] = *(const u32x4*)(sl + (((4 * h + c) ^ sw) << 4));
    SCHED_FENCE();
#pragma unroll
    for (int c = 0; c < 4; ++c) {
      if (n < 8) { piece(n, 2 * c); if (c < 3) piece(n, 2 * c + 1); }
      SCHED_FENCE();
#pragma unroll
      for (int i = 0; i < 4; ++i) {
        const float lo = bf_lo(u[c][i]), hi = bf_hi(u[c][i]);
        add_f32(sum0, lo); add_f32(sum1, hi); fma_sq(sq0, lo); fma_sq(sq1, hi);
      }
      SCHED_FENCE();
    }
  };
  WAIT_VM(14); STAMP(2); BARRIER();
  step(0, 3); WAIT_VM(14); WAIT_LGKM0(); BARRIER();
  step(1, 4); WAIT_VM(14); WAIT_LGKM0(); BARRIER();
  step(2, 5); WAIT_VM(14); WAIT_LGKM0(); BARRIER();
  step(3, 6); WAIT_VM(14); WAIT_LGKM0(); BARRIER();
  step(4, 7); WAIT_VM(14); WAIT_LGKM0(); BARRIER();
  if (prep) { prep_issue(); step(5, 8); WAIT_VM(11); } else { step(5, 8); WAIT_VM(7); }
  WAIT_LGKM0(); BARRIER();
  step(6, 8); if (prep) WAIT_VM(4); else WAIT_VM(0);
  WAIT_LGKM0(); BARRIER();
  step(7, 8);
  STAMP(3);
  const float s1 = half_swap_sum(sum0 + sum1), s2 = half_swap_sum(sq0 + sq1);
  const float mean = s1 * (1.0f / D);
  const float var = fmaxf(s2 * (1.0f / D) - mean * mean, 0.f);
  const float rstd = 1.0f / sqrtf(var + LN_EPS);
  f32x2* st = (f32x2*)(smem + P1_AUX + 2048);
  if (h == 0) st[row] = f32x2{rstd, mean * rstd};
  WAIT_LGKM0(); BARRIER();
  BARRIER();
  STAMP(4);
  if (prep) {
    WAIT_VM(0);
    const int blk = (lane >> 4) & 1, q = (lane & 15) >> 2, pp = lane & 3;
    const char* bs = stg + blk * 2048 + (4 * h + q) * 128 + (pp >> 1) * 16 + 8 * (pp & 1);
#pragma unroll
    for (int i = 0; i < 4; ++i) {
      const int fs = 4 * cg + i, jj = fs / 7, t = fs % 7;
      const bf16x8 f = tr_frag(bs + i * 32, bs + 8 * 128 + i * 32);
      st16_p1(make_rsrc(ws, WS_BYTES), lane * 16, WS_WGU + ((jj * 32 + pks) * 7 + t) * 1024, as_u4(f));
      const float c = half_swap_sum(colsum_frag(f, 0.f));
      if (h == 0) *(float*)(ws + WS_CS + ((size_t)pks * 112 + fs) * 128 + r * 4) = c;
    }
  }
  STAMP(5); PROF_END(0, ws);
}

__global__ __launch_bounds__(512, 2) void k_qkv(const char* __restrict__ x, const char* __restrict__ wqkv, const char* __restrict__ wg,
                                                const char* __restrict__ wu, char* __restrict__ ws) {
  extern __shared__ __attribute__((aligned(16))) char smem[];
  const int w = __builtin_amdgcn_readfirstlane(threadIdx.x >> 6), lane = threadIdx.x & 63;
  const int xcd = blockIdx.x & 7, i = blockIdx.x >> 3;
  const int p = 4 * (xcd & 3) + (i & 3), j = 8 * (xcd >> 2) + (i >> 2);
  if (blockIdx.x == 255 && threadIdx.x < 48) *(unsigned*)(ws + WS_CNT + threadIdx.x * 128) = 0u;
  if (w >= 4) p1_helper(smem, w - 4, lane, p, j, x, wqkv, wg, wu, ws);
  else if (j <= 9) p1_compute<0>(smem, w, lane, p, j, ws);
  else if (j == 10) p1_compute<4>(smem, w, lane, p, j, ws);
  else p1_compute<7>(smem, w, lane, p, j, ws);
}

constexpr int P2_K = 0, P2_V = 24576, P2_VP = 1040, P2_Q = P2_V + 24 * P2_VP;
constexpr int L_WO = 77824;
constexpr int L_RED = 143360;
constexpr int L_CS = L_RED + 16384;
constexpr int MAIN_LDS = 163840;

template <bool FULL>
DI void attn_task(const char* smem, int lane, int hh, int wq, int qb, rsrc_t rws, int head) {
  const int r = lane & 31, h = lane >> 5;
  const char* kb_ = smem + P2_K + (hh * 12 + wq) * 1024 + lane * 16;
  const bool valid = (r >> 4) == hh;
  const char* vb_ = smem + P2_V + 2 * wq * P2_VP + (valid ? lane * 16 : 1024);
  const bf16x8 qf = lds_frag(smem + P2_Q + (hh * 4 + wq) * 1024 + lane * 16);
  const f32x16 zero = {};
  const int t0 = FULL ? 1 : ((8 - qb) > 1 ? (8 - qb) : 1);
  const bool have_first = FULL || qb >= 8;
  f32x16 sm;
  {
    const f32x16 sd = MFMA32(lds_frag(kb_ + 8 * 1024), qf, zero);
    f32x16 sf;
    if (have_first) sf = MFMA32(lds_frag(kb_), qf, zero);
    else { for (int i = 0; i < 16; ++i) sf[i] = -1e30f; }
#pragma unroll
    for (int i = 0; i < 16; ++i) { const int jj = (i & 3) + 8 * (i >> 2) + 4 * h; sm[i] = jj <= r ? sd[i] : sf[i]; }
  }
  float m = sm[0];
#pragma unroll
  for (int i = 1; i < 16; ++i) m = fmaxf(m, sm[i]);
  if (FULL) {
#pragma unroll
    for (int t = 1; t < 8; ++t) {
      const f32x16 s = MFMA32(lds_frag(kb_ + t * 1024), qf, zero);
#pragma unroll
      for (int i = 0; i < 16; i += 2) m = fmaxf(fmaxf(m, s[i]), s[i + 1]);
    }
  } else {
    for (int t = t0; t < 8; ++t) {
      const f32x16 s = MFMA32(lds_frag(kb_ + t * 1024), qf, zero);
#pragma unroll
      for (int i = 0; i < 16; i += 2) m = fmaxf(fmaxf(m, s[i]), s[i + 1]);
    }
  }
  m = half_swap_max(m);
  f32x16 negm;
#pragma unroll
  for (int i = 0; i < 16; ++i) negm[i] = -m;
  f32x16 o = zero;
  {
    float pv[16];
#pragma unroll
    for (int i = 0; i < 16; ++i) pv[i] = __builtin_amdgcn_exp2f(sm[i] - m);
    u32x4 pa = __builtin_bit_cast(u32x4, pack8(pv[0], pv[1], pv[2], pv[3], pv[4], pv[5], pv[6], pv[7]));
    u32x4 pb = __builtin_bit_cast(u32x4, pack8(pv[8], pv[9], pv[10], pv[11], pv[12], pv[13], pv[14], pv[15]));
    u32x4 da, db;
#pragma unroll
    for (int i = 0; i < 4; ++i) {
      const int ja = ((2 * i) & 3) + 8 * ((2 * i) >> 2) + 4 * h;
      const unsigned ma = (ja <= r ? 0xffffu : 0u) | (ja + 1 <= r ? 0xffff0000u : 0u);
      const unsigned mb = (ja + 16 <= r ? 0xffffu : 0u) | (ja + 17 <= r ? 0xffff0000u : 0u);
      da[i] = pa[i] & ma; db[i] = pb[i] & mb;
    }
    o = MFMA32(lds_frag(vb_ + 16 * P2_VP), __builtin_bit_cast(bf16x8, da), o);
    o = MFMA32(lds_frag(vb_ + 17 * P2_VP), __builtin_bit_cast(bf16x8, db), o);
    if (have_first) {
      o = MFMA32(lds_frag(vb_), __builtin_bit_cast(bf16x8, pa ^ da), o);
      o = MFMA32(lds_frag(vb_ + P2_VP), __builtin_bit_cast(bf16x8, pb ^ db), o);
    }
  }
  auto tile = [&](int t) {
    const f32x16 s = MFMA32(lds_frag(kb_ + t * 1024), qf, negm);
    float pv[16];
#pragma unroll
    for (int i = 0; i < 16; ++i) pv[i] = __builtin_amdgcn_exp2f(s[i]);
    o = MFMA32(lds_frag(vb_ + (2 * t) * P2_VP), pack8(pv[0], pv[1], pv[2], pv[3], pv[4], pv[5], pv[6], pv[7]), o);
    o = MFMA32(lds_frag(vb_ + (2 * t + 1) * P2_VP), pack8(pv[8], pv[9], pv[10], pv[11], pv[12], pv[13], pv[14], pv[15]), o);
  };
  if (FULL) {
#pragma unroll
    for (int t = 1; t < 8; ++t) tile(t);
  } else {
    for (int t = t0; t < 8; ++t) tile(t);
  }
  const float l = hh ? o[0] : o[8];
  const float inv = 1.0f / l;
  float ov[8];
#pragma unroll
  for (int i = 0; i < 8; ++i) ov[i] = (hh ? o[8 + i] : o[i]) * inv;
  st16_dev(rws, lane * 16, WS_O + (((qb >> 1) * 32 + head) * 2 + (qb & 1)) * 1024, as_u4(pack8(ov[0], ov[1], ov[2], ov[3], ov[4], ov[5], ov[6], ov[7])));
}

DI unsigned wrow_voff(int lane) { return (lane >> 3) * 1024 + (((lane & 7) ^ (((lane >> 4) & 1) << 2)) << 4); }
DI unsigned wtr_off(int lane, int nt) {
  const int h = lane >> 5, blk = (lane >> 4) & 1, q = (lane & 15) >> 2, pp = lane & 3, sw = (q >> 1) & 1;
  return (4 * h + q) * 128 + (2 * blk + (pp >> 1) + 4 * (nt ^ sw)) * 16 + 8 * (pp & 1);
}

DI void phase_attn(char* smem, int w, int lane, char* ws, rsrc_t rws, const char* wo PROF_ARG) {
  const int xcd = blockIdx.x & 7, i = blockIdx.x >> 3;
  const int hp = 2 * xcd + (i & 1), p = i >> 1;
  const int kb0 = 4 * p - 8;
  const int hh = w >> 2, wq = w & 3, qb = 4 * p + wq, head = 2 * hp + hh;
  blds16(rws, lane * 16, WS_Q + (head * 64 + qb) * 1024, smem + P2_Q + w * 1024);
#pragma unroll
  for (int n = 0; n < 3; ++n) {
    const int t = wq * 3 + n, sv = w * 3 + n;
    const int kt = kb0 + t < 0 ? 0 : kb0 + t, vt = 2 * kb0 + sv < 0 ? 0 : 2 * kb0 + sv;
    blds16(rws, lane * 16, WS_K + (head * 64 + kt) * 1024, smem + P2_K + (hh * 12 + t) * 1024);
    blds16(rws, lane * 16, WS_V + (hp * 128 + vt) * 1024, smem + P2_V + sv * P2_VP);
  }
  if (threadIdx.x < 24) *(u32x4*)(smem + P2_V + threadIdx.x * P2_VP + 1024) = u32x4{0x3f803f80u, 0x3f803f80u, 0x3f803f80u, 0x3f803f80u};
  WAIT_VM(0); WAIT_LGKM0(); BARRIER();
  STAMP(2);
  {
    const rsrc_t rwo = make_rsrc(wo, D * D * 2);
    const int c = i >> 2;
    const unsigned vo = wrow_voff(lane);
#pragma unroll
    for (int n = 0; n < 8; ++n) blds16(rwo, vo, (8 * w + n) * 8192 + c * 128, smem + L_WO + (8 * w + n) * 1024);
  }
  if (qb >= 8) attn_task<true>(smem, lane, hh, wq, qb, rws, head);
  else attn_task<false>(smem, lane, hh, wq, qb, rws, head);
  WAIT_VM(0); WAIT_LGKM0(); BARRIER();
  if (threadIdx.x == 0) signal_panel(ws, 0, p);
}

template <int NACC>
DI f32x16 pair_reduce(char* smem, int w, int lane, const f32x16 (&acc)[NACC]) {
  const int kh = w >> 1;
  const f32x16 send = kh ? acc[0] : acc[1];
  f32x16 a = kh ? acc[1] : acc[0];
#pragma unroll
  for (int g = 0; g < 4; ++g)
    *(f32x4*)(smem + L_RED + (w * 4 + g) * 1024 + lane * 16) = f32x4{send[4 * g], send[4 * g + 1], send[4 * g + 2], send[4 * g + 3]};
  WAIT_LGKM0(); BARRIER();
#pragma unroll
  for (int g = 0; g < 4; ++g) {
    const f32x4 v = *(const f32x4*)(smem + L_RED + ((w ^ 2) * 4 + g) * 1024 + lane * 16);
    a[4 * g] += v[0]; a[4 * g + 1] += v[1]; a[4 * g + 2] += v[2]; a[4 * g + 3] += v[3];
  }
  return a;
}

DI void oproj_compute(char* smem, int w, int lane, char* ws, rsrc_t rws, const char* x, int tp, int c PROF_ARG) {
  const int r = lane & 31, h = lane >> 5, tt = w & 1, kh = w >> 1;
  const int tb = 2 * tp + tt, ntile = 2 * c + kh;
  u32x2 rx[4];
#pragma unroll
  for (int g = 0; g < 4; ++g) rx[g] = *(const u32x2*)(x + (size_t)(32 * tb + r) * 1024 + (32 * ntile + 8 * g + 4 * h) * 2);
  if (w == 0) wait_panel(ws, 0, tp >> 1);
  BARRIER();
  STAMP(4);
  u32x4 xf[16];
#pragma unroll
  for (int g = 0; g < 16; ++g) xf[g] = ld16_dev(rws, lane * 16, WS_O + ((tp * 32 + 16 * kh + g) * 2 + tt) * 1024);
  const unsigned lb = lds_addr(smem) + L_WO + kh * 32768;
  const unsigned a0 = opaque(lb + wtr_off(lane, 0)), a1 = opaque(lb + wtr_off(lane, 1));
  bf16x8 wf[3][2];
  auto rd = [&](int g) {
    wf[g % 3][0] = tr_frag_a(a0 + g * 2048, a0 + g * 2048 + 1024);
    wf[g % 3][1] = tr_frag_a(a1 + g * 2048, a1 + g * 2048 + 1024);
  };
  f32x16 acc[2] = {};
  rd(0); rd(1);
  SCHED_FENCE();
#pragma unroll
  for (int g = 0; g < 16; ++g) {
    acc[0] = MFMA32(wf[g % 3][0], as_frag(xf[g]), acc[0]); SCHED_FENCE();
    if (g + 2 < 16) { rd(g + 2); SCHED_FENCE(); }
    acc[1] = MFMA32(wf[g % 3][1], as_frag(xf[g]), acc[1]); SCHED_FENCE();
  }
  STAMP(5);
  f32x16 a = pair_reduce<2>(smem, w, lane, acc);
#pragma unroll
  for (int g = 0; g < 4; ++g) {
    a[4 * g] += bf_lo(rx[g][0]); a[4 * g + 1] += bf_hi(rx[g][0]); a[4 * g + 2] += bf_lo(rx[g][1]); a[4 * g + 3] += bf_hi(rx[g][1]);
  }
  float s1 = 0.f, s2 = 0.f;
#pragma unroll
  for (int s = 0; s < 2; ++s) {
    const u32x4 u = as_u4(pack8(a[8 * s], a[8 * s + 1], a[8 * s + 2], a[8 * s + 3], a[8 * s + 4], a[8 * s + 5], a[8 * s + 6], a[8 * s + 7]));
    st16_dev(rws, lane * 16, WS_H + (tb * 32 + 2 * ntile + s) * 1024, u);
#pragma unroll
    for (int i = 0; i < 4; ++i) { const float lo = bf_lo(u[i]), hi = bf_hi(u[i]); s1 += lo + hi; s2 += lo * lo + hi * hi; }
  }
  s1 = half_swap_sum(s1); s2 = half_swap_sum(s2);
  if (h == 0) __builtin_amdgcn_raw_buffer_store_b64(u32x2{__float_as_uint(s1), __float_as_uint(s2)}, rws, r * 8, WS_ST + (tb * 16 + ntile) * 256, SC1);
  WAIT_VM(0); BARRIER();
  if (threadIdx.x == 0) signal_panel(ws, 1, tp >> 1);
}

#define XD4 12
constexpr int P4_SLOT = 28672, P4_XD = XD4;

DI void gateup_compute(char* smem, int w, int lane, char* ws, rsrc_t rws, int p, int j PROF_ARG) {
  const int r = lane & 31, h = lane >> 5, tb = 4 * p + w;
  if (w == 0) wait_panel(ws, 1, p);
  BARRIER();
  STAMP(7);
  u32x4 xf[32];
  auto ldx = [&](int g) { xf[g] = ld16_dev(rws, lane * 16, WS_H + (tb * 32 + g) * 1024); };
  ldx(0); ldx(1);
  u32x2 sv[8];
#pragma unroll
  for (int i = 0; i < 8; ++i) sv[i] = __builtin_amdgcn_raw_buffer_load_b64(rws, (8 * h + i) * 256 + r * 8, WS_ST + tb * 4096, SC1);
#pragma unroll
  for (int g = 2; g < P4_XD; ++g) ldx(g);
  const unsigned lb = lds_addr(smem) + lane * 16;
  unsigned ba[3];
#pragma unroll
  for (int b = 0; b < 3; ++b) ba[b] = opaque(lb + b * 2 * P4_SLOT);
  bf16x8 wf[2][7];
  auto rdw = [&](int g, int t) {
    const int sl = (g >> 2) % 5;
    wf[g & 1][t] = lds_frag_a(ba[sl >> 1] + (sl & 1) * P4_SLOT + ((g & 3) * 7 + t) * 1024);
  };
  f32x16 acc[7] = {};
#pragma unroll
  for (int t = 0; t < 7; ++t) rdw(0, t);
  float s1 = 0.f, s2 = 0.f;
  SCHED_FENCE();
#pragma unroll
  for (int g = 0; g < 32; ++g) {
    const int n = g + 1;
#pragma unroll
    for (int t = 0; t < 7; ++t) {
      acc[t] = MFMA32(wf[g & 1][t], as_frag(xf[g]), acc[t]); SCHED_FENCE();
      if (t == 0 && n < 32 && (n & 3) == 0) { WAIT_LGKM0(); BARRIER(); }
      if (t == 0 && n == 32) { WAIT_LGKM0(); BARRIER(); }
      if (n < 32) rdw(n, t);
      if (t == 1 && g + P4_XD < 32) ldx(g + P4_XD);
      if (g == 2 && t == 2) {
#pragma unroll
        for (int i = 0; i < 8; ++i) { const unsigned e0 = sv[i][0], e1 = sv[i][1]; s1 += __uint_as_float(e0); s2 += __uint_as_float(e1); }
      }
      SCHED_FENCE();
    }
  }
  STAMP(8);
  s1 = half_swap_sum(s1); s2 = half_swap_sum(s2);
  const float mean = s1 * (1.0f / D);
  const float rstd = 1.0f / sqrtf(fmaxf(s2 * (1.0f / D) - mean * mean, 0.f) + LN_EPS);
  const float mr = mean * rstd;
  const float* cst = (const float*)(smem + L_CS);
  const unsigned dst = WS_M + (((tb >> 1) * 112 + 7 * j) * 2 + (tb & 1)) * 1024;
#pragma unroll
  for (int t = 0; t < 7; ++t) {
    float v[8];
#pragma unroll
    for (int g = 0; g < 2; ++g) {
      const f32x4 cg = *(const f32x4*)(cst + 32 * t + 8 * g + 4 * h), cu = *(const f32x4*)(cst + 32 * t + 16 + 8 * g + 4 * h);
#pragma unroll
      for (int i = 0; i < 4; ++i) {
        const float gv = acc[t][4 * g + i] * rstd - mr * cg[i], uv = acc[t][8 + 4 * g + i] * rstd - mr * cu[i];
        v[4 * g + i] = fmaxf(gv, 0.f) * uv;
      }
    }
    st16_dev(rws, lane * 16, dst + t * 2048, as_u4(pack8(v[0], v[1], v[2], v[3], v[4], v[5], v[6], v[7])));
  }
  WAIT_VM(0); BARRIER();
  if (threadIdx.x == 0) signal_panel(ws, 2, p);
}

#define XD5 10
constexpr int P5_SLOT = 32768, P5_XD = XD5;
DI constexpr int p5_slot(int it) { return ((it + 3) & 3) * P5_SLOT; }

DI int p5_rot(int it, int rot) { const int v = it + rot; return v >= 7 ? v - 7 : v; }

DI void down_compute(char* smem, int w, int lane, char* ws, rsrc_t rws, char* out, int tp, int c, int rot PROF_ARG) {
  const int r = lane & 31, h = lane >> 5, tt = w & 1, kh = w >> 1;
  const int tb = 2 * tp + tt, ntile = 2 * c + kh;
  u32x4 rh[2];
#pragma unroll
  for (int s = 0; s < 2; ++s) rh[s] = ld16_dev(rws, lane * 16, WS_H + (tb * 32 + 2 * ntile + s) * 1024);
  if (w == 0) wait_panel(ws, 2, tp >> 1);
  BARRIER();
  STAMP(10);
  u32x4 xf[56];
  unsigned mb[7];
#pragma unroll
  for (int it = 0; it < 7; ++it) mb[it] = WS_M + ((tp * 112 + 56 * kh + 8 * p5_rot(it, rot)) * 2 + tt) * 1024;
  auto ldx = [&](int g) { xf[g] = ld16_dev(rws, lane * 16, mb[g >> 3] + (g & 7) * 2048); };
#pragma unroll
  for (int g = 0; g < P5_XD; ++g) ldx(g);
  const unsigned lb = lds_addr(smem) + kh * 16384;
  unsigned a0[2], a1[2];
#pragma unroll
  for (int b = 0; b < 2; ++b) { a0[b] = opaque(lb + wtr_off(lane, 0) + b * 65536); a1[b] = opaque(lb + wtr_off(lane, 1) + b * 65536); }
  bf16x8 wf[3][2];
  auto rd = [&](int g) {
    const int o = p5_slot(g >> 3) + (g & 7) * 2048, b = o >> 16, oo = o & 65535;
    wf[g % 3][0] = tr_frag_a(a0[b] + oo, a0[b] + oo + 1024);
    wf[g % 3][1] = tr_frag_a(a1[b] + oo, a1[b] + oo + 1024);
  };
  f32x16 acc[2] = {};
  rd(0); rd(1);
  SCHED_FENCE();
#pragma unroll
  for (int g = 0; g < 56; ++g) {
    const int n = g + 2;
    acc[0] = MFMA32(wf[g % 3][0], as_frag(xf[g]), acc[0]); SCHED_FENCE();
    if (n < 56) {
      if ((n & 7) == 0) { WAIT_LGKM0(); BARRIER(); }
      rd(n);
    }
    if (g + P5_XD < 56) ldx(g + P5_XD);
    SCHED_FENCE();
    acc[1] = MFMA32(wf[g % 3][1], as_frag(xf[g]), acc[1]); SCHED_FENCE();
  }
  STAMP(11);
  f32x16 a = pair_reduce<2>(smem, w, lane, acc);
  unsigned pk[8];
#pragma unroll
  for (int s = 0; s < 2; ++s) {
#pragma unroll
    for (int i = 0; i < 4; ++i) { a[8 * s + 2 * i] += bf_lo(rh[s][i]); a[8 * s + 2 * i + 1] += bf_hi(rh[s][i]); }
    const u32x4 u = as_u4(pack8(a[8 * s], a[8 * s + 1], a[8 * s + 2], a[8 * s + 3], a[8 * s + 4], a[8 * s + 5], a[8 * s + 6], a[8 * s + 7]));
#pragma unroll
    for (int i = 0; i < 4; ++i) pk[4 * s + i] = u[i];
  }
  char* dst = out + (size_t)(32 * tb + r) * 1024 + (32 * ntile + 8 * h) * 2;
#pragma unroll
  for (int s = 0; s < 2; ++s) {
    auto q0 = __builtin_amdgcn_permlane32_swap(pk[4 * s], pk[4 * s + 2], false, false);
    auto q1 = __builtin_amdgcn_permlane32_swap(pk[4 * s + 1], pk[4 * s + 3], false, false);
    *(u32x4*)(dst + s * 32) = u32x4{q0[0], q1[0], q0[1], q1[1]};
  }
}

DI void loader_345(char* smem, int lw, int lane, char* ws, rsrc_t rws, const char* wd, int j, int c, int rot PROF_ARG) {
  {
    f32x4 a = {0.f, 0.f, 0.f, 0.f};
    if (lane < 56) {
      f32x4 v[8];
#pragma unroll
      for (int i = 0; i < 8; ++i) v[i] = *(const f32x4*)(ws + WS_CS + ((size_t)(8 * lw + i) * 112 + 7 * j) * 128 + lane * 16);
#pragma unroll
      for (int i = 0; i < 8; ++i) a += v[i];
      *(f32x4*)(smem + L_CS + lw * 896 + lane * 16) = a;
    }
    WAIT_VM(0);
  }
  auto wgu = [&](int s) {
#pragma unroll
    for (int i = 0; i < 7; ++i) blds16(rws, lane * 16, WS_WGU + (j * 32 * 7 + s * 28 + lw * 7 + i) * 1024, smem + (s % 5) * P4_SLOT + (lw * 7 + i) * 1024);
  };
  const rsrc_t rwd = make_rsrc(wd, F * D * 2);
  const unsigned vo = wrow_voff(lane);
  auto wdn = [&](int it) {
#pragma unroll
    for (int i = 0; i < 8; ++i) {
      const int pc = lw * 8 + i, kh = pc >> 4, rr = (pc & 15) * 8;
      blds16(rwd, vo, (896 * kh + 128 * p5_rot(it, rot) + rr) * 1024 + c * 128, smem + p5_slot(it) + pc * 1024);
    }
  };
  wgu(0); wgu(1);
  WAIT_LGKM0(); BARRIER();
  BARRIER();
  wgu(2); wgu(3); wgu(4);
  if (lw == 0 && lane < 56) {
    const char* t = smem + L_CS + lane * 16;
    *(f32x4*)(smem + L_CS + lane * 16) = *(const f32x4*)t + *(const f32x4*)(t + 896) + *(const f32x4*)(t + 1792) + *(const f32x4*)(t + 2688);
  }
  WAIT_LGKM0(); BARRIER();
  STAMP(6);
  WAIT_VM(28); BARRIER();
  WAIT_VM(21); BARRIER(); wgu(5);
  WAIT_VM(21); BARRIER(); wgu(6);
  WAIT_VM(21); BARRIER(); wgu(7);
  WAIT_VM(21); BARRIER();
  WAIT_VM(14); BARRIER(); wdn(0);
  WAIT_VM(15); BARRIER();
  WAIT_VM(8); BARRIER(); wdn(1);
  BARRIER(); wdn(2); wdn(3);
  BARRIER();
  STAMP(9);
  WAIT_VM(24); BARRIER();
  WAIT_VM(16); BARRIER(); wdn(4);
  WAIT_VM(16); BARRIER(); wdn(5);
  WAIT_VM(16); BARRIER(); wdn(6);
  WAIT_VM(16); BARRIER();
  WAIT_VM(8); BARRIER();
  WAIT_VM(0); BARRIER();
  BARRIER();
}

__global__ __launch_bounds__(512, 2) void k_main(char* __restrict__ ws, const char* __restrict__ x, const char* __restrict__ wo,
                                                 const char* __restrict__ wd, char* __restrict__ out) {
  extern __shared__ __attribute__((aligned(16))) char smem[];
  PROF_BEGIN
  const int w = __builtin_amdgcn_readfirstlane(threadIdx.x >> 6), lane = threadIdx.x & 63;
  const rsrc_t rws = make_rsrc(ws, WS_BYTES);
  phase_attn(smem, w, lane, ws, rws, wo PROF_PASS);
  STAMP(3);
  const int xcd = blockIdx.x & 7, i = blockIdx.x >> 3;
  const int tp = 4 * xcd + (i & 3), c = i >> 2;
  const int p4 = 8 * (xcd & 1) + (i & 7), j4 = 4 * (xcd >> 1) + (i >> 3);
  const int rot = (c + 2 * (i & 3)) % 7;
  if (w >= 4) loader_345(smem, w - 4, lane, ws, rws, wd, j4, c, rot PROF_PASS);
  else {
    oproj_compute(smem, w, lane, ws, rws, x, tp, c PROF_PASS);
    STAMP(6);
    gateup_compute(smem, w, lane, ws, rws, p4, j4 PROF_PASS);
    STAMP(9);
    down_compute(smem, w, lane, ws, rws, out, tp, c, rot PROF_PASS);
  }
  STAMP(1); PROF_END(1, ws);
}

extern "C" void kernel_launch(void* const* d_in, const int* in_sizes, int n_in, void* d_out, int out_size, void* d_ws, size_t ws_size,
                              hipStream_t stream) {
  const char* x = (const char*)d_in[0];
  const char* wqkv = (const char*)d_in[1];
  const char* wo = (const char*)d_in[2];
  const char* wg = (const char*)d_in[4];
  const char* wu = (const char*)d_in[5];
  const char* wd = (const char*)d_in[6];
  char* ws = (char*)d_ws;
  (void)hipFuncSetAttribute((const void*)k_qkv, hipFuncAttributeMaxDynamicSharedMemorySize, P1_LDS);
  (void)hipFuncSetAttribute((const void*)k_main, hipFuncAttributeMaxDynamicSharedMemorySize, MAIN_LDS);
  k_qkv<<<256, 512, P1_LDS, stream>>>(x, wqkv, wg, wu, ws);
  k_main<<<256, 512, MAIN_LDS, stream>>>(ws, x, wo, wd, (char*)d_out);
}
```

```cpp
#include <hip/hip_runtime.h>
#include <hip/hip_bf16.h>
#include <stdint.h>

typedef __attribute__((ext_vector_type(8))) __bf16 bf16x8;
typedef __attribute__((ext_vector_type(2))) __bf16 bf16x2;
typedef __attribute__((ext_vector_type(16))) float f32x16;
typedef __attribute__((ext_vector_type(8))) float f32x8;
typedef __attribute__((ext_vector_type(4))) float f32x4;
typedef __attribute__((ext_vector_type(2))) float f32x2;
typedef __attribute__((ext_vector_type(4))) unsigned u32x4;
typedef __attribute__((ext_vector_type(2))) unsigned u32x2;
typedef __attribute__((ext_vector_type(4))) short s16x4;
typedef __attribute__((address_space(3))) s16x4* lds_s16x4;
typedef __attribute__((address_space(3))) void* lds_vp;
typedef const __attribute__((address_space(1))) void* g_vp;

#define DI __device__ __forceinline__
#define MFMA32(a, b, c) __builtin_amdgcn_mfma_f32_32x32x16_bf16((a), (b), (c), 0, 0, 0)
#define BARRIER() asm volatile("s_barrier" ::: "memory")
#define WAIT_VM(n) asm volatile("s_waitcnt vmcnt(" #n ")" ::: "memory")
#define WAIT_LGKM0() asm volatile("s_waitcnt lgkmcnt(0)" ::: "memory")
#define SCHED_FENCE() __builtin_amdgcn_sched_barrier(0)

constexpr int S = 2048, D = 512, F = 1792;
constexpr float LN_EPS = 1e-5f;
constexpr float Q_SCALE = 0.25f * 1.4426950408889634f;

constexpr unsigned WS_Q = 0;
constexpr unsigned WS_K = WS_Q + (2u << 20);
constexpr unsigned WS_V = WS_K + (2u << 20);
constexpr unsigned WS_O = WS_V + (2u << 20);
constexpr unsigned WS_H = WS_O + (2u << 20);
constexpr unsigned WS_ST = WS_H + (2u << 20);
constexpr unsigned WS_M = WS_ST + (256u << 10);
constexpr unsigned WS_WGU = WS_M + (7u << 20);
constexpr unsigned WS_CS = WS_WGU + (3584u << 10);
constexpr unsigned WS_CNT = WS_CS + (448u << 10);
constexpr unsigned WS_BYTES = WS_CNT + 48 * 128;

typedef __amdgpu_buffer_rsrc_t rsrc_t;
typedef __attribute__((address_space(3))) const bf16x8* lds_fp;
typedef __attribute__((address_space(1))) unsigned gu32;
constexpr int SC1 = 16;
constexpr unsigned QKV_CODE_BYTES = 33280, MAIN_CODE_BYTES = 25856;

DI rsrc_t make_rsrc(const void* p, unsigned bytes) { return __builtin_amdgcn_make_buffer_rsrc((void*)p, 0, bytes, 0x00020000); }
DI void glds16(const void* g, void* l) { __builtin_amdgcn_global_load_lds((g_vp)g, (lds_vp)l, 16, 0, 0); }
DI void blds16(rsrc_t rs, unsigned voff, unsigned soff, char* l) { __builtin_amdgcn_raw_ptr_buffer_load_lds(rs, (lds_vp)l, 16, voff, soff, 0, 0); }
DI u32x4 ld16_dev(rsrc_t rs, unsigned voff, unsigned soff) { return __builtin_amdgcn_raw_buffer_load_b128(rs, voff, soff, SC1); }
DI void st16_dev(rsrc_t rs, unsigned voff, unsigned soff, u32x4 v) { __builtin_amdgcn_raw_buffer_store_b128(v, rs, voff + soff, 0, SC1); }
#define P1_AUXBITS 16
DI void st16_p1(rsrc_t rs, unsigned voff, unsigned soff, u32x4 v) { __builtin_amdgcn_raw_buffer_store_b128(v, rs, voff + soff, 0, P1_AUXBITS); }
DI void touch(const char* g, char* scratch) { __builtin_amdgcn_global_load_lds((g_vp)g, (lds_vp)scratch, 4, 0, 0); }
#define KERNEL_ADDR(dst, sym) do { unsigned lo_, hi_; \
  asm volatile("s_getpc_b64 s[40:41]\n\ts_add_u32 s40, s40, " #sym "@rel32@lo+4\n\ts_addc_u32 s41, s41, " #sym "@rel32@hi+12\n\ts_mov_b32 %0, s40\n\ts_mov_b32 %1, s41" \
               : "=s"(lo_), "=s"(hi_) :: "s40", "s41", "scc"); \
  dst = ((unsigned long long)hi_ << 32) | lo_; } while (0)
DI bf16x8 lds_frag(const char* p) { return *(const bf16x8*)p; }
DI bf16x8 lds_frag_a(unsigned a) { return *(lds_fp)(size_t)a; }
DI unsigned lds_addr(const char* p) { return (unsigned)(size_t)(const __attribute__((address_space(3))) char*)p; }
DI unsigned opaque(unsigned v) { asm volatile("" : "+v"(v)); return v; }
DI bf16x8 tr_frag(const char* p0, const char* p1) {
  s16x4 lo = __builtin_amdgcn_ds_read_tr16_b64_v4i16((lds_s16x4)p0);
  s16x4 hi = __builtin_amdgcn_ds_read_tr16_b64_v4i16((lds_s16x4)p1);
  return __builtin_bit_cast(bf16x8, __builtin_shufflevector(lo, hi, 0, 1, 2, 3, 4, 5, 6, 7));
}
DI bf16x8 tr_frag_a(unsigned a0, unsigned a1) {
  s16x4 lo = __builtin_amdgcn_ds_read_tr16_b64_v4i16((lds_s16x4)(size_t)a0);
  s16x4 hi = __builtin_amdgcn_ds_read_tr16_b64_v4i16((lds_s16x4)(size_t)a1);
  return __builtin_bit_cast(bf16x8, __builtin_shufflevector(lo, hi, 0, 1, 2, 3, 4, 5, 6, 7));
}
DI bf16x8 pack8(float a0, float a1, float a2, float a3, float a4, float a5, float a6, float a7) {
  f32x8 v = {a0, a1, a2, a3, a4, a5, a6, a7};
  return __builtin_convertvector(v, bf16x8);
}
DI u32x4 as_u4(bf16x8 v) { return __builtin_bit_cast(u32x4, v); }
DI bf16x8 as_frag(u32x4 v) { return __builtin_bit_cast(bf16x8, v); }
DI float bf_lo(unsigned u) { return __uint_as_float(u << 16); }
DI float bf_hi(unsigned u) { return __uint_as_float(u & 0xffff0000u); }
DI float half_swap_sum(float v) {
  auto r = __builtin_amdgcn_permlane32_swap(__float_as_uint(v), __float_as_uint(v), false, false);
  return __uint_as_float(r[0]) + __uint_as_float(r[1]);
}
DI float half_swap_max(float v) {
  auto r = __builtin_amdgcn_permlane32_swap(__float_as_uint(v), __float_as_uint(v), false, false);
  return fmaxf(__uint_as_float(r[0]), __uint_as_float(r[1]));
}
DI void add_f32(float& a, float v) { asm("v_add_f32 %0, %0, %1" : "+v"(a) : "v"(v)); }
DI void fma_sq(float& a, float v) { asm("v_fma_f32 %0, %1, %1, %0" : "+v"(a) : "v"(v)); }
DI float colsum_frag(bf16x8 f, float acc) {
  const bf16x2 ones = {(__bf16)1.0f, (__bf16)1.0f};
  const u32x4 u = as_u4(f);
#pragma unroll
  for (int i = 0; i < 4; ++i) { const unsigned e = u[i]; acc = __builtin_amdgcn_fdot2_f32_bf16(__builtin_bit_cast(bf16x2, e), ones, acc, false); }
  return acc;
}

DI void signal_panel(char* ws, int phase, int panel) {
  __hip_atomic_fetch_add((gu32*)(ws + WS_CNT + (phase * 16 + panel) * 128), 1u, __ATOMIC_RELAXED, __HIP_MEMORY_SCOPE_AGENT);
}
DI void wait_panel(char* ws, int phase, int panel) {
  gu32* word = (gu32*)(ws + WS_CNT + (phase * 16 + panel) * 128);
  for (unsigned spins = 0; spins < (1u << 19); ++spins) {
    if ((unsigned)__builtin_amdgcn_readfirstlane(__hip_atomic_load(word, __ATOMIC_RELAXED, __HIP_MEMORY_SCOPE_AGENT)) >= 16u) break;
  }
  asm volatile("" ::: "memory");
}
constexpr int P1_SLOT = 28672, P1_NSLOT = 5, P1_AUX = P1_SLOT * P1_NSLOT, P1_PREP = P1_AUX + 4096;
constexpr int P1_LDS = P1_PREP + 16384;

template <int VMASK>
DI void p1_compute(char* smem, int w, int lane, int p, int j, char* ws) {
  const int r = lane & 31, h = lane >> 5;
  const int row = 32 * w + r, sw = (r >> 1) & 7;
  const unsigned lb = lds_addr(smem);
  unsigned xa[4][3], wa[3];
#pragma unroll
  for (int ks = 0; ks < 4; ++ks) {
    const unsigned o = lb + row * 128 + (((2 * ks + h) ^ sw) << 4);
#pragma unroll
    for (int b = 0; b < 3; ++b) xa[ks][b] = opaque(o + b * 2 * P1_SLOT);
  }
  {
    const int q = (lane & 15) >> 2, pp = lane & 3, blk = (lane >> 4) & 1;
    const unsigned o = lb + 16384 + (8 * h + q) * 192 + (2 * blk + (pp >> 1)) * 16 + 8 * (pp & 1);
#pragma unroll
    for (int b = 0; b < 3; ++b) wa[b] = opaque(o + b * 2 * P1_SLOT);
  }
  f32x16 acc[3] = {};
  float cs[3] = {0.f, 0.f, 0.f};
  bf16x8 xf[3], wf[3][3];
  auto rdx = [&](int g) { const int sl = (g >> 2) % P1_NSLOT; xf[g % 3] = lds_frag_a(xa[g & 3][sl >> 1] + (sl & 1) * P1_SLOT); };
  auto rdw = [&](int g, int c) {
    const int sl = (g >> 2) % P1_NSLOT;
    const unsigned a = wa[sl >> 1] + (sl & 1) * P1_SLOT + (16 * (g & 3)) * 192 + c * 64;
    wf[g % 3][c] = tr_frag_a(a, a + 4 * 192);
  };
  auto mm = [&](int g, int c) {
    if ((VMASK >> c) & 1) acc[c] = MFMA32(xf[g % 3], wf[g % 3][c], acc[c]);
    else acc[c] = MFMA32(wf[g % 3][c], xf[g % 3], acc[c]);
  };
  BARRIER();
#pragma unroll
  for (int g = 0; g < 2; ++g) { rdx(g); rdw(g, 0); rdw(g, 1); rdw(g, 2); }
  SCHED_FENCE();
#pragma unroll
  for (int g = 0; g < 32; ++g) {
    const int n = g + 2;
    mm(g, 0); SCHED_FENCE();
    if (n < 32) {
      if ((n & 3) == 0) { WAIT_LGKM0(); BARRIER(); }
      rdx(n); rdw(n, 0); SCHED_FENCE();
    }
    mm(g, 1); SCHED_FENCE();
    if (n < 32) { rdw(n, 1); rdw(n, 2); SCHED_FENCE(); }
    mm(g, 2); SCHED_FENCE();
    if ((g & 3) == w) {
#pragma unroll
      for (int c = 0; c < 3; ++c) cs[c] = colsum_frag(wf[g % 3][c], cs[c]);
    }
    SCHED_FENCE();
  }
  float* csp = (float*)(smem + P1_AUX);
  float* cst = csp + 384;
  f32x2* st = (f32x2*)(smem + P1_AUX + 2048);
#pragma unroll
  for (int c = 0; c < 3; ++c) { float t = half_swap_sum(cs[c]); if (h == 0) csp[w * 96 + c * 32 + r] = t; }
  WAIT_LGKM0(); BARRIER();
  if (w < 2) { int t = w * 64 + lane; if (t < 96) cst[t] = csp[t] + csp[96 + t] + csp[192 + t] + csp[288 + t]; }
  WAIT_LGKM0(); BARRIER();
  const int tb = 4 * p + w;
  const rsrc_t rws = make_rsrc(ws, WS_BYTES);
  const f32x2 mine = st[row];
#pragma unroll
  for (int c = 0; c < 3; ++c) {
    const int c0 = 96 * j + 32 * c;
    if ((VMASK >> c) & 1) {
      const float csv = cst[32 * c + r];
      float v[16];
#pragma unroll
      for (int i = 0; i < 16; ++i) { f32x2 t = st[32 * w + (i & 3) + 8 * (i >> 2) + 4 * h]; v[i] = acc[c][i] * t[0] - t[1] * csv; }
      const unsigned dst = WS_V + (((c0 - 1024) >> 5) * 128 + 2 * tb) * 1024;
      st16_p1(rws, lane * 16, dst, as_u4(pack8(v[0], v[1], v[2], v[3], v[4], v[5], v[6], v[7])));
      st16_p1(rws, lane * 16, dst + 1024, as_u4(pack8(v[8], v[9], v[10], v[11], v[12], v[13], v[14], v[15])));
    } else {
      const bool isq = c0 < 512;
      const float sc = isq ? Q_SCALE : 1.0f;
      const float a = mine[0] * sc, b = mine[1] * sc;
      float v[16];
#pragma unroll
      for (int g = 0; g < 4; ++g) {
        f32x4 cv = *(const f32x4*)(cst + 32 * c + 8 * g + 4 * h);
#pragma unroll
        for (int i = 0; i < 4; ++i) v[4 * g + i] = acc[c][4 * g + i] * a - b * cv[i];
      }
      const int head = (c0 & 511) >> 4;
      const unsigned dst = (isq ? WS_Q : WS_K) + (head * 64 + tb) * 1024;
      st16_p1(rws, lane * 16, dst, as_u4(pack8(v[0], v[1], v[2], v[3], v[4], v[5], v[6], v[7])));
      st16_p1(rws, lane * 16, dst + 65536, as_u4(pack8(v[8], v[9], v[10], v[11], v[12], v[13], v[14], v[15])));
    }
  }
}

DI void p1_helper(char* smem, int lw, int lane, int p, int j, const char* x, const char* wqkv, const char* wg, const char* wu, char* ws) {
  const rsrc_t rx = make_rsrc(x, S * D * 2), rw = make_rsrc(wqkv, D * 3 * D * 2);
  unsigned xo[4], wo_[3];
#pragma unroll
  for (int i = 0; i < 4; ++i) {
    int row = 32 * lw + 8 * i + (lane >> 3);
    xo[i] = (128 * p + row) * 1024 + (((lane & 7) ^ ((row >> 1) & 7)) << 4);
  }
#pragma unroll
  for (int i = 0; i < 3; ++i) { int g = 64 * (3 * lw + i) + lane; int row = g / 12, c = g % 12; wo_[i] = row * 3072 + 192 * j + c * 16; }
  auto issue = [&](int s) {
    char* sl = smem + (s % P1_NSLOT) * P1_SLOT;
#pragma unroll
    for (int i = 0; i < 4; ++i) blds16(rx, xo[i], s * 128, sl + (4 * lw + i) * 1024);
#pragma unroll
    for (int i = 0; i < 3; ++i) blds16(rw, wo_[i], s * 64 * 3072, sl + 16384 + (3 * lw + i) * 1024);
  };
  issue(0); issue(1); issue(2);
  const int b = blockIdx.x, cg = b % 28, pks = 4 * (b / 28) + lw;
  const bool prep = b < 224;
  char* stg = smem + P1_PREP + lw * 4096;
  auto prep_issue = [&]() {
    const rsrc_t rg = make_rsrc(wg, D * F * 2), ru = make_rsrc(wu, D * F * 2);
    const unsigned off = (16 * pks + (lane >> 3)) * 3584 + cg * 128 + (lane & 7) * 16;
    blds16(rg, off, 0, stg); blds16(rg, off, 8 * 3584, stg + 1024);
    blds16(ru, off, 0, stg + 2048); blds16(ru, off, 8 * 3584, stg + 3072);
  };
  const int r = lane & 31, h = lane >> 5, row = 32 * lw + r, sw = (r >> 1) & 7;
  float sum0 = 0.f, sum1 = 0.f, sq0 = 0.f, sq1 = 0.f;
  auto piece = [&](int s, int i) {
    char* sl = smem + (s % P1_NSLOT) * P1_SLOT;
    if (i < 4) blds16(rx, xo[i], s * 128, sl + (4 * lw + i) * 1024);
    else blds16(rw, wo_[i - 4], s * 64 * 3072, sl + 16384 + (3 * lw + i - 4) * 1024);
  };
  auto step = [&](int s, int n) {
    const char* sl = smem + (s % P1_NSLOT) * P1_SLOT + row * 128;
    u32x4 u[4];
#pragma unroll
    for (int c = 0; c < 4; ++c) u[c] = *(const u32x4*)(sl + (((4 * h + c) ^ sw) << 4));
    SCHED_FENCE();
#pragma unroll
    for (int c = 0; c < 4; ++c) {
      if (n < 8) { piece(n, 2 * c); if (c < 3) piece(n, 2 * c + 1); }
      SCHED_FENCE();
#pragma unroll
      for (int i = 0; i < 4; ++i) {
        const float lo = bf_lo(u[c][i]), hi = bf_hi(u[c][i]);
        add_f32(sum0, lo); add_f32(sum1, hi); fma_sq(sq0, lo); fma_sq(sq1, hi);
      }
      SCHED_FENCE();
    }
  };
  WAIT_VM(14); BARRIER();
  step(0, 3); WAIT_VM(14); WAIT_LGKM0(); BARRIER();
  step(1, 4); WAIT_VM(14); WAIT_LGKM0(); BARRIER();
  step(2, 5); WAIT_VM(14); WAIT_LGKM0(); BARRIER();
  step(3, 6); WAIT_VM(14); WAIT_LGKM0(); BARRIER();
  step(4, 7); WAIT_VM(14); WAIT_LGKM0(); BARRIER();
  if (prep) { prep_issue(); step(5, 8); WAIT_VM(11); } else { step(5, 8); WAIT_VM(7); }
  WAIT_LGKM0(); BARRIER();
  step(6, 8); if (prep) WAIT_VM(4); else WAIT_VM(0);
  WAIT_LGKM0(); BARRIER();
  step(7, 8);
  const float s1 = half_swap_sum(sum0 + sum1), s2 = half_swap_sum(sq0 + sq1);
  const float mean = s1 * (1.0f / D);
  const float var = fmaxf(s2 * (1.0f / D) - mean * mean, 0.f);
  const float rstd = 1.0f / sqrtf(var + LN_EPS);
  f32x2* st = (f32x2*)(smem + P1_AUX + 2048);
  if (h == 0) st[row] = f32x2{rstd, mean * rstd};
  WAIT_LGKM0(); BARRIER();
  BARRIER();
  if (prep) {
    WAIT_VM(0);
    const int blk = (lane >> 4) & 1, q = (lane & 15) >> 2, pp = lane & 3;
    const char* bs = stg + blk * 2048 + (4 * h + q) * 128 + (pp >> 1) * 16 + 8 * (pp & 1);
#pragma unroll
    for (int i = 0; i < 4; ++i) {
      const int fs = 4 * cg + i, jj = fs / 7, t = fs % 7;
      const bf16x8 f = tr_frag(bs + i * 32, bs + 8 * 128 + i * 32);
      st16_p1(make_rsrc(ws, WS_BYTES), lane * 16, WS_WGU + ((jj * 32 + pks) * 7 + t) * 1024, as_u4(f));
      const float c = half_swap_sum(colsum_frag(f, 0.f));
      if (h == 0) *(float*)(ws + WS_CS + ((size_t)pks * 112 + fs) * 128 + r * 4) = c;
    }
  }
}

extern "C" __global__ __launch_bounds__(512, 2) void k_qkv(const char* __restrict__ x, const char* __restrict__ wqkv, const char* __restrict__ wg,
                                                const char* __restrict__ wu, char* __restrict__ ws,
                                                const char* __restrict__ wo, const char* __restrict__ wd, const char* __restrict__ out) {
  extern __shared__ __attribute__((aligned(16))) char smem[];
  const int w = __builtin_amdgcn_readfirstlane(threadIdx.x >> 6), lane = threadIdx.x & 63;
  const int xcd = blockIdx.x & 7, i = blockIdx.x >> 3;
  const int p = 4 * (xcd & 3) + (i & 3), j = 8 * (xcd >> 2) + (i >> 2);
  if (blockIdx.x == 255 && threadIdx.x < 48) *(unsigned*)(ws + WS_CNT + threadIdx.x * 128) = 0u;
  if (w < 4) {
    char* scr = smem + P1_AUX + 3072;
    {
      unsigned long long ka, kb;
      KERNEL_ADDR(ka, k_qkv); KERNEL_ADDR(kb, k_main);
#pragma unroll
      for (int n = 0; n < 2; ++n) { const unsigned o = ((w * 2 + n) * 64 + lane) * 128; if (o < QKV_CODE_BYTES) touch((const char*)ka + o, scr); }
      { const unsigned o = (w * 64 + lane) * 128; if (o < MAIN_CODE_BYTES) touch((const char*)kb + o, scr); }
    }
  }
  if (w >= 4) p1_helper(smem, w - 4, lane, p, j, x, wqkv, wg, wu, ws);
  else if (j <= 9) p1_compute<0>(smem, w, lane, p, j, ws);
  else if (j == 10) p1_compute<4>(smem, w, lane, p, j, ws);
  else p1_compute<7>(smem, w, lane, p, j, ws);
}

constexpr int P2_K = 0, P2_V = 24576, P2_VP = 1040, P2_Q = P2_V + 24 * P2_VP;
constexpr int L_WO = 77824;
constexpr int L_RED = 143360;
constexpr int L_CS = L_RED + 16384;
constexpr int MAIN_LDS = 163840;

template <bool FULL>
DI void attn_task(const char* smem, int lane, int hh, int wq, int qb, rsrc_t rws, int head) {
  const int r = lane & 31, h = lane >> 5;
  const char* kb_ = smem + P2_K + (hh * 12 + wq) * 1024 + lane * 16;
  const bool valid = (r >> 4) == hh;
  const char* vb_ = smem + P2_V + 2 * wq * P2_VP + (valid ? lane * 16 : 1024);
  const bf16x8 qf = lds_frag(smem + P2_Q + (hh * 4 + wq) * 1024 + lane * 16);
  const f32x16 zero = {};
  const int t0 = FULL ? 1 : ((8 - qb) > 1 ? (8 - qb) : 1);
  const bool have_first = FULL || qb >= 8;
  f32x16 sm;
  {
    const f32x16 sd = MFMA32(lds_frag(kb_ + 8 * 1024), qf, zero);
    f32x16 sf;
    if (have_first) sf = MFMA32(lds_frag(kb_), qf, zero);
    else { for (int i = 0; i < 16; ++i) sf[i] = -1e30f; }
#pragma unroll
    for (int i = 0; i < 16; ++i) { const int jj = (i & 3) + 8 * (i >> 2) + 4 * h; sm[i] = jj <= r ? sd[i] : sf[i]; }
  }
  float m = sm[0];
#pragma unroll
  for (int i = 1; i < 16; ++i) m = fmaxf(m, sm[i]);
  if (FULL) {
#pragma unroll
    for (int t = 1; t < 8; ++t) {
      const f32x16 s = MFMA32(lds_frag(kb_ + t * 1024), qf, zero);
#pragma unroll
      for (int i = 0; i < 16; i += 2) m = fmaxf(fmaxf(m, s[i]), s[i + 1]);
    }
  } else {
    for (int t = t0; t < 8; ++t) {
      const f32x16 s = MFMA32(lds_frag(kb_ + t * 1024), qf, zero);
#pragma unroll
      for (int i = 0; i < 16; i += 2) m = fmaxf(fmaxf(m, s[i]), s[i + 1]);
    }
  }
  m = half_swap_max(m);
  f32x16 negm;
#pragma unroll
  for (int i = 0; i < 16; ++i) negm[i] = -m;
  f32x16 o = zero;
  {
    float pv[16];
#pragma unroll
    for (int i = 0; i < 16; ++i) pv[i] = __builtin_amdgcn_exp2f(sm[i] - m);
    u32x4 pa = __builtin_bit_cast(u32x4, pack8(pv[0], pv[1], pv[2], pv[3], pv[4], pv[5], pv[6], pv[7]));
    u32x4 pb = __builtin_bit_cast(u32x4, pack8(pv[8], pv[9], pv[10], pv[11], pv[12], pv[13], pv[14], pv[15]));
    u32x4 da, db;
#pragma unroll
    for (int i = 0; i < 4; ++i) {
      const int ja = ((2 * i) & 3) + 8 * ((2 * i) >> 2) + 4 * h;
      const unsigned ma = (ja <= r ? 0xffffu : 0u) | (ja + 1 <= r ? 0xffff0000u : 0u);
      const unsigned mb = (ja + 16 <= r ? 0xffffu : 0u) | (ja + 17 <= r ? 0xffff0000u : 0u);
      da[i] = pa[i] & ma; db[i] = pb[i] & mb;
    }
    o = MFMA32(lds_frag(vb_ + 16 * P2_VP), __builtin_bit_cast(bf16x8, da), o);
    o = MFMA32(lds_frag(vb_ + 17 * P2_VP), __builtin_bit_cast(bf16x8, db), o);
    if (have_first) {
      o = MFMA32(lds_frag(vb_), __builtin_bit_cast(bf16x8, pa ^ da), o);
      o = MFMA32(lds_frag(vb_ + P2_VP), __builtin_bit_cast(bf16x8, pb ^ db), o);
    }
  }
  auto tile = [&](int t) {
    const f32x16 s = MFMA32(lds_frag(kb_ + t * 1024), qf, negm);
    float pv[16];
#pragma unroll
    for (int i = 0; i < 16; ++i) pv[i] = __builtin_amdgcn_exp2f(s[i]);
    o = MFMA32(lds_frag(vb_ + (2 * t) * P2_VP), pack8(pv[0], pv[1], pv[2], pv[3], pv[4], pv[5], pv[6], pv[7]), o);
    o = MFMA32(lds_frag(vb_ + (2 * t + 1) * P2_VP), pack8(pv[8], pv[9], pv[10], pv[11], pv[12], pv[13], pv[14], pv[15]), o);
  };
  if (FULL) {
#pragma unroll
    for (int t = 1; t < 8; ++t) tile(t);
  } else {
    for (int t = t0; t < 8; ++t) tile(t);
  }
  const float l = hh ? o[0] : o[8];
  const float inv = 1.0f / l;
  float ov[8];
#pragma unroll
  for (int i = 0; i < 8; ++i) ov[i] = (hh ? o[8 + i] : o[i]) * inv;
  st16_dev(rws, lane * 16, WS_O + (((qb >> 1) * 32 + head) * 2 + (qb & 1)) * 1024, as_u4(pack8(ov[0], ov[1], ov[2], ov[3], ov[4], ov[5], ov[6], ov[7])));
}

DI unsigned wrow_voff(int lane) { return (lane >> 3) * 1024 + (((lane & 7) ^ (((lane >> 4) & 1) << 2)) << 4); }
DI unsigned wtr_off(int lane, int nt) {
  const int h = lane >> 5, blk = (lane >> 4) & 1, q = (lane & 15) >> 2, pp = lane & 3, sw = (q >> 1) & 1;
  return (4 * h + q) * 128 + (2 * blk + (pp >> 1) + 4 * (nt ^ sw)) * 16 + 8 * (pp & 1);
}

DI void phase_attn(char* smem, int w, int lane, char* ws, rsrc_t rws, const char* wo) {
  const int xcd = blockIdx.x & 7, i = blockIdx.x >> 3;
  const int hp = 2 * xcd + (i & 1), p = i >> 1;
  const int kb0 = 4 * p - 8;
  const int hh = w >> 2, wq = w & 3, qb = 4 * p + wq, head = 2 * hp + hh;
  blds16(rws, lane * 16, WS_Q + (head * 64 + qb) * 1024, smem + P2_Q + w * 1024);
#pragma unroll
  for (int n = 0; n < 3; ++n) {
    const int t = wq * 3 + n, sv = w * 3 + n;
    const int kt = kb0 + t < 0 ? 0 : kb0 + t, vt = 2 * kb0 + sv < 0 ? 0 : 2 * kb0 + sv;
    blds16(rws, lane * 16, WS_K + (head * 64 + kt) * 1024, smem + P2_K + (hh * 12 + t) * 1024);
    blds16(rws, lane * 16, WS_V + (hp * 128 + vt) * 1024, smem + P2_V + sv * P2_VP);
  }
  if (threadIdx.x < 24) *(u32x4*)(smem + P2_V + threadIdx.x * P2_VP + 1024) = u32x4{0x3f803f80u, 0x3f803f80u, 0x3f803f80u, 0x3f803f80u};
  WAIT_VM(0); WAIT_LGKM0(); BARRIER();
  {
    const rsrc_t rwo = make_rsrc(wo, D * D * 2);
    const int c = i >> 2;
    const unsigned vo = wrow_voff(lane);
#pragma unroll
    for (int n = 0; n < 8; ++n) blds16(rwo, vo, (8 * w + n) * 8192 + c * 128, smem + L_WO + (8 * w + n) * 1024);
  }
  if (qb >= 8) attn_task<true>(smem, lane, hh, wq, qb, rws, head);
  else attn_task<false>(smem, lane, hh, wq, qb, rws, head);
  WAIT_VM(0); WAIT_LGKM0(); BARRIER();
  if (threadIdx.x == 0) signal_panel(ws, 0, p);
}

template <int NACC>
DI f32x16 pair_reduce(char* smem, int w, int lane, const f32x16 (&acc)[NACC]) {
  const int kh = w >> 1;
  const f32x16 send = kh ? acc[0] : acc[1];
  f32x16 a = kh ? acc[1] : acc[0];
#pragma unroll
  for (int g = 0; g < 4; ++g)
    *(f32x4*)(smem + L_RED + (w * 4 + g) * 1024 + lane * 16) = f32x4{send[4 * g], send[4 * g + 1], send[4 * g + 2], send[4 * g + 3]};
  WAIT_LGKM0(); BARRIER();
#pragma unroll
  for (int g = 0; g < 4; ++g) {
    const f32x4 v = *(const f32x4*)(smem + L_RED + ((w ^ 2) * 4 + g) * 1024 + lane * 16);
    a[4 * g] += v[0]; a[4 * g + 1] += v[1]; a[4 * g + 2] += v[2]; a[4 * g + 3] += v[3];
  }
  return a;
}

DI void oproj_compute(char* smem, int w, int lane, char* ws, rsrc_t rws, const char* x, int tp, int c) {
  const int r = lane & 31, h = lane >> 5, tt = w & 1, kh = w >> 1;
  const int tb = 2 * tp + tt, ntile = 2 * c + kh;
  u32x2 rx[4];
#pragma unroll
  for (int g = 0; g < 4; ++g) rx[g] = *(const u32x2*)(x + (size_t)(32 * tb + r) * 1024 + (32 * ntile + 8 * g + 4 * h) * 2);
  if (w == 0) wait_panel(ws, 0, tp >> 1);
  BARRIER();
  u32x4 xf[16];
#pragma unroll
  for (int g = 0; g < 16; ++g) xf[g] = ld16_dev(rws, lane * 16, WS_O + ((tp * 32 + 16 * kh + g) * 2 + tt) * 1024);
  const unsigned lb = lds_addr(smem) + L_WO + kh * 32768;
  const unsigned a0 = opaque(lb + wtr_off(lane, 0)), a1 = opaque(lb + wtr_off(lane, 1));
  bf16x8 wf[3][2];
  auto rd = [&](int g) {
    wf[g % 3][0] = tr_frag_a(a0 + g * 2048, a0 + g * 2048 + 1024);
    wf[g % 3][1] = tr_frag_a(a1 + g * 2048, a1 + g * 2048 + 1024);
  };
  f32x16 acc[2] = {};
  rd(0); rd(1);
  SCHED_FENCE();
#pragma unroll
  for (int g = 0; g < 16; ++g) {
    acc[0] = MFMA32(wf[g % 3][0], as_frag(xf[g]), acc[0]); SCHED_FENCE();
    if (g + 2 < 16) { rd(g + 2); SCHED_FENCE(); }
    acc[1] = MFMA32(wf[g % 3][1], as_frag(xf[g]), acc[1]); SCHED_FENCE();
  }
  f32x16 a = pair_reduce<2>(smem, w, lane, acc);
#pragma unroll
  for (int g = 0; g < 4; ++g) {
    a[4 * g] += bf_lo(rx[g][0]); a[4 * g + 1] += bf_hi(rx[g][0]); a[4 * g + 2] += bf_lo(rx[g][1]); a[4 * g + 3] += bf_hi(rx[g][1]);
  }
  float s1 = 0.f, s2 = 0.f;
#pragma unroll
  for (int s = 0; s < 2; ++s) {
    const u32x4 u = as_u4(pack8(a[8 * s], a[8 * s + 1], a[8 * s + 2], a[8 * s + 3], a[8 * s + 4], a[8 * s + 5], a[8 * s + 6], a[8 * s + 7]));
    st16_dev(rws, lane * 16, WS_H + (tb * 32 + 2 * ntile + s) * 1024, u);
#pragma unroll
    for (int i = 0; i < 4; ++i) { const float lo = bf_lo(u[i]), hi = bf_hi(u[i]); s1 += lo + hi; s2 += lo * lo + hi * hi; }
  }
  s1 = half_swap_sum(s1); s2 = half_swap_sum(s2);
  if (h == 0) __builtin_amdgcn_raw_buffer_store_b64(u32x2{__float_as_uint(s1), __float_as_uint(s2)}, rws, r * 8, WS_ST + (tb * 16 + ntile) * 256, SC1);
  WAIT_VM(0); BARRIER();
  if (threadIdx.x == 0) signal_panel(ws, 1, tp >> 1);
}

#define XD4 12
constexpr int P4_SLOT = 28672, P4_XD = XD4;

DI void gateup_compute(char* smem, int w, int lane, char* ws, rsrc_t rws, int p, int j) {
  const int r = lane & 31, h = lane >> 5, tb = 4 * p + w;
  if (w == 0) wait_panel(ws, 1, p);
  BARRIER();
  u32x4 xf[32];
  auto ldx = [&](int g) { xf[g] = ld16_dev(rws, lane * 16, WS_H + (tb * 32 + g) * 1024); };
  ldx(0); ldx(1);
  u32x2 sv[8];
#pragma unroll
  for (int i = 0; i < 8; ++i) sv[i] = __builtin_amdgcn_raw_buffer_load_b64(rws, (8 * h + i) * 256 + r * 8, WS_ST + tb * 4096, SC1);
#pragma unroll
  for (int g = 2; g < P4_XD; ++g) ldx(g);
  const unsigned lb = lds_addr(smem) + lane * 16;
  unsigned ba[3];
#pragma unroll
  for (int b = 0; b < 3; ++b) ba[b] = opaque(lb + b * 2 * P4_SLOT);
  bf16x8 wf[2][7];
  auto rdw = [&](int g, int t) {
    const int sl = (g >> 2) % 5;
    wf[g & 1][t] = lds_frag_a(ba[sl >> 1] + (sl & 1) * P4_SLOT + ((g & 3) * 7 + t) * 1024);
  };
  f32x16 acc[7] = {};
#pragma unroll
  for (int t = 0; t < 7; ++t) rdw(0, t);
  float s1 = 0.f, s2 = 0.f;
  SCHED_FENCE();
#pragma unroll
  for (int g = 0; g < 32; ++g) {
    const int n = g + 1;
#pragma unroll
    for (int t = 0; t < 7; ++t) {
      acc[t] = MFMA32(wf[g & 1][t], as_frag(xf[g]), acc[t]); SCHED_FENCE();
      if (t == 0 && n < 32 && (n & 3) == 0) { WAIT_LGKM0(); BARRIER(); }
      if (t == 0 && n == 32) { WAIT_LGKM0(); BARRIER(); }
      if (n < 32) rdw(n, t);
      if (t == 1 && g + P4_XD < 32) ldx(g + P4_XD);
      if (g == 2 && t == 2) {
#pragma unroll
        for (int i = 0; i < 8; ++i) { const unsigned e0 = sv[i][0], e1 = sv[i][1]; s1 += __uint_as_float(e0); s2 += __uint_as_float(e1); }
      }
      SCHED_FENCE();
    }
  }
  s1 = half_swap_sum(s1); s2 = half_swap_sum(s2);
  const float mean = s1 * (1.0f / D);
  const float rstd = 1.0f / sqrtf(fmaxf(s2 * (1.0f / D) - mean * mean, 0.f) + LN_EPS);
  const float mr = mean * rstd;
  const float* cst = (const float*)(smem + L_CS);
  const unsigned dst = WS_M + (((tb >> 1) * 112 + 7 * j) * 2 + (tb & 1)) * 1024;
#pragma unroll
  for (int t = 0; t < 7; ++t) {
    float v[8];
#pragma unroll
    for (int g = 0; g < 2; ++g) {
      const f32x4 cg = *(const f32x4*)(cst + 32 * t + 8 * g + 4 * h), cu = *(const f32x4*)(cst + 32 * t + 16 + 8 * g + 4 * h);
#pragma unroll
      for (int i = 0; i < 4; ++i) {
        const float gv = acc[t][4 * g + i] * rstd - mr * cg[i], uv = acc[t][8 + 4 * g + i] * rstd - mr * cu[i];
        v[4 * g + i] = fmaxf(gv, 0.f) * uv;
      }
    }
    st16_dev(rws, lane * 16, dst + t * 2048, as_u4(pack8(v[0], v[1], v[2], v[3], v[4], v[5], v[6], v[7])));
  }
  WAIT_VM(0); BARRIER();
  if (threadIdx.x == 0) signal_panel(ws, 2, p);
}

#define XD5 10
constexpr int P5_SLOT = 32768, P5_XD = XD5;
DI constexpr int p5_slot(int it) { return ((it + 3) & 3) * P5_SLOT; }

DI int p5_rot(int it, int rot) { const int v = it + rot; return v >= 7 ? v - 7 : v; }

DI void down_compute(char* smem, int w, int lane, char* ws, rsrc_t rws, char* out, int tp, int c, int rot) {
  const int r = lane & 31, h = lane >> 5, tt = w & 1, kh = w >> 1;
  const int tb = 2 * tp + tt, ntile = 2 * c + kh;
  u32x4 rh[2];
#pragma unroll
  for (int s = 0; s < 2; ++s) rh[s] = ld16_dev(rws, lane * 16, WS_H + (tb * 32 + 2 * ntile + s) * 1024);
  if (w == 0) wait_panel(ws, 2, tp >> 1);
  BARRIER();
  u32x4 xf[56];
  unsigned mb[7];
#pragma unroll
  for (int it = 0; it < 7; ++it) mb[it] = WS_M + ((tp * 112 + 56 * kh + 8 * p5_rot(it, rot)) * 2 + tt) * 1024;
  auto ldx = [&](int g) { xf[g] = ld16_dev(rws, lane * 16, mb[g >> 3] + (g & 7) * 2048); };
#pragma unroll
  for (int g = 0; g < P5_XD; ++g) ldx(g);
  const unsigned lb = lds_addr(smem) + kh * 16384;
  unsigned a0[2], a1[2];
#pragma unroll
  for (int b = 0; b < 2; ++b) { a0[b] = opaque(lb + wtr_off(lane, 0) + b * 65536); a1[b] = opaque(lb + wtr_off(lane, 1) + b * 65536); }
  bf16x8 wf[3][2];
  auto rd = [&](int g) {
    const int o = p5_slot(g >> 3) + (g & 7) * 2048, b = o >> 16, oo = o & 65535;
    wf[g % 3][0] = tr_frag_a(a0[b] + oo, a0[b] + oo + 1024);
    wf[g % 3][1] = tr_frag_a(a1[b] + oo, a1[b] + oo + 1024);
  };
  f32x16 acc[2] = {};
  rd(0); rd(1);
  SCHED_FENCE();
#pragma unroll
  for (int g = 0; g < 56; ++g) {
    const int n = g + 2;
    acc[0] = MFMA32(wf[g % 3][0], as_frag(xf[g]), acc[0]); SCHED_FENCE();
    if (n < 56) {
      if ((n & 7) == 0) { WAIT_LGKM0(); BARRIER(); }
      rd(n);
    }
    if (g + P5_XD < 56) ldx(g + P5_XD);
    SCHED_FENCE();
    acc[1] = MFMA32(wf[g % 3][1], as_frag(xf[g]), acc[1]); SCHED_FENCE();
  }
  f32x16 a = pair_reduce<2>(smem, w, lane, acc);
  unsigned pk[8];
#pragma unroll
  for (int s = 0; s < 2; ++s) {
#pragma unroll
    for (int i = 0; i < 4; ++i) { a[8 * s + 2 * i] += bf_lo(rh[s][i]); a[8 * s + 2 * i + 1] += bf_hi(rh[s][i]); }
    const u32x4 u = as_u4(pack8(a[8 * s], a[8 * s + 1], a[8 * s + 2], a[8 * s + 3], a[8 * s + 4], a[8 * s + 5], a[8 * s + 6], a[8 * s + 7]));
#pragma unroll
    for (int i = 0; i < 4; ++i) pk[4 * s + i] = u[i];
  }
  char* dst = out + (size_t)(32 * tb + r) * 1024 + (32 * ntile + 8 * h) * 2;
#pragma unroll
  for (int s = 0; s < 2; ++s) {
    auto q0 = __builtin_amdgcn_permlane32_swap(pk[4 * s], pk[4 * s + 2], false, false);
    auto q1 = __builtin_amdgcn_permlane32_swap(pk[4 * s + 1], pk[4 * s + 3], false, false);
    *(u32x4*)(dst + s * 32) = u32x4{q0[0], q1[0], q0[1], q1[1]};
  }
}

DI void loader_345(char* smem, int lw, int lane, char* ws, rsrc_t rws, const char* wd, int j, int c, int rot) {
  {
    f32x4 a = {0.f, 0.f, 0.f, 0.f};
    if (lane < 56) {
      f32x4 v[8];
#pragma unroll
      for (int i = 0; i < 8; ++i) v[i] = *(const f32x4*)(ws + WS_CS + ((size_t)(8 * lw + i) * 112 + 7 * j) * 128 + lane * 16);
#pragma unroll
      for (int i = 0; i < 8; ++i) a += v[i];
      *(f32x4*)(smem + L_CS + lw * 896 + lane * 16) = a;
    }
    WAIT_VM(0);
  }
  auto wgu = [&](int s) {
#pragma unroll
    for (int i = 0; i < 7; ++i) blds16(rws, lane * 16, WS_WGU + (j * 32 * 7 + s * 28 + lw * 7 + i) * 1024, smem + (s % 5) * P4_SLOT + (lw * 7 + i) * 1024);
  };
  const rsrc_t rwd = make_rsrc(wd, F * D * 2);
  const unsigned vo = wrow_voff(lane);
  auto wdn = [&](int it) {
#pragma unroll
    for (int i = 0; i < 8; ++i) {
      const int pc = lw * 8 + i, kh = pc >> 4, rr = (pc & 15) * 8;
      blds16(rwd, vo, (896 * kh + 128 * p5_rot(it, rot) + rr) * 1024 + c * 128, smem + p5_slot(it) + pc * 1024);
    }
  };
  wgu(0); wgu(1);
  WAIT_LGKM0(); BARRIER();
  BARRIER();
  wgu(2); wgu(3); wgu(4);
  if (lw == 0 && lane < 56) {
    const char* t = smem + L_CS + lane * 16;
    *(f32x4*)(smem + L_CS + lane * 16) = *(const f32x4*)t + *(const f32x4*)(t + 896) + *(const f32x4*)(t + 1792) + *(const f32x4*)(t + 2688);
  }
  WAIT_LGKM0(); BARRIER();
  WAIT_VM(28); BARRIER();
  WAIT_VM(21); BARRIER(); wgu(5);
  WAIT_VM(21); BARRIER(); wgu(6);
  WAIT_VM(21); BARRIER(); wgu(7);
  WAIT_VM(21); BARRIER();
  WAIT_VM(14); BARRIER(); wdn(0);
  WAIT_VM(15); BARRIER();
  WAIT_VM(8); BARRIER(); wdn(1);
  BARRIER(); wdn(2); wdn(3);
  BARRIER();
  WAIT_VM(24); BARRIER();
  WAIT_VM(16); BARRIER(); wdn(4);
  WAIT_VM(16); BARRIER(); wdn(5);
  WAIT_VM(16); BARRIER(); wdn(6);
  WAIT_VM(16); BARRIER();
  WAIT_VM(8); BARRIER();
  WAIT_VM(0); BARRIER();
  BARRIER();
}

extern "C" __global__ __launch_bounds__(512, 2) void k_main(char* __restrict__ ws, const char* __restrict__ x, const char* __restrict__ wo,
                                                 const char* __restrict__ wd, char* __restrict__ out) {
  extern __shared__ __attribute__((aligned(16))) char smem[];
  const int w = __builtin_amdgcn_readfirstlane(threadIdx.x >> 6), lane = threadIdx.x & 63;
  const rsrc_t rws = make_rsrc(ws, WS_BYTES);
  phase_attn(smem, w, lane, ws, rws, wo);
  const int xcd = blockIdx.x & 7, i = blockIdx.x >> 3;
  const int tp = 4 * xcd + (i & 3), c = i >> 2;
  const int p4 = 8 * (xcd & 1) + (i & 7), j4 = 4 * (xcd >> 1) + (i >> 3);
  const int rot = (c + 2 * (i & 3)) % 7;
  if (w >= 4) loader_345(smem, w - 4, lane, ws, rws, wd, j4, c, rot);
  else {
    oproj_compute(smem, w, lane, ws, rws, x, tp, c);
    gateup_compute(smem, w, lane, ws, rws, p4, j4);
    down_compute(smem, w, lane, ws, rws, out, tp, c, rot);
  }
}

extern "C" void kernel_launch(void* const* d_in, const int* in_sizes, int n_in, void* d_out, int out_size, void* d_ws, size_t ws_size,
                              hipStream_t stream) {
  const char* x = (const char*)d_in[0];
  const char* wqkv = (const char*)d_in[1];
  const char* wo = (const char*)d_in[2];
  const char* wg = (const char*)d_in[4];
  const char* wu = (const char*)d_in[5];
  const char* wd = (const char*)d_in[6];
  char* ws = (char*)d_ws;
  (void)hipFuncSetAttribute((const void*)k_qkv, hipFuncAttributeMaxDynamicSharedMemorySize, P1_LDS);
  (void)hipFuncSetAttribute((const void*)k_main, hipFuncAttributeMaxDynamicSharedMemorySize, MAIN_LDS);
  k_qkv<<<256, 512, P1_LDS, stream>>>(x, wqkv, wg, wu, ws, wo, wd, (const char*)d_out);
  k_main<<<256, 512, MAIN_LDS, stream>>>(ws, x, wo, wd, (char*)d_out);
}
```

```cpp
#include <hip/hip_runtime.h>
#include <hip/hip_bf16.h>
#include <stdint.h>

typedef __attribute__((ext_vector_type(8))) __bf16 bf16x8;
typedef __attribute__((ext_vector_type(2))) __bf16 bf16x2;
typedef __attribute__((ext_vector_type(16))) float f32x16;
typedef __attribute__((ext_vector_type(8))) float f32x8;
typedef __attribute__((ext_vector_type(4))) float f32x4;
typedef __attribute__((ext_vector_type(2))) float f32x2;
typedef __attribute__((ext_vector_type(4))) unsigned u32x4;
typedef __attribute__((ext_vector_type(2))) unsigned u32x2;
typedef __attribute__((ext_vector_type(4))) short s16x4;
typedef __attribute__((address_space(3))) s16x4* lds_s16x4;
typedef __attribute__((address_space(3))) void* lds_vp;
typedef const __attribute__((address_space(1))) void* g_vp;

#define POLL_GAP "4"
#define POLL_SLEEP ""
#define DI __device__ __forceinline__
#define MFMA32(a, b, c) __builtin_amdgcn_mfma_f32_32x32x16_bf16((a), (b), (c), 0, 0, 0)
#define BARRIER() asm volatile("s_barrier" ::: "memory")
#define WAIT_VM(n) asm volatile("s_waitcnt vmcnt(" #n ")" ::: "memory")
#define WAIT_LGKM0() asm volatile("s_waitcnt lgkmcnt(0)" ::: "memory")
#define SCHED_FENCE() __builtin_amdgcn_sched_barrier(0)

constexpr int S = 2048, D = 512, F = 1792;
constexpr float LN_EPS = 1e-5f;
constexpr float Q_SCALE = 0.25f * 1.4426950408889634f;

constexpr unsigned WS_Q = 0;
constexpr unsigned WS_K = WS_Q + (2u << 20);
constexpr unsigned WS_V = WS_K + (2u << 20);
constexpr unsigned WS_O = WS_V + (2u << 20);
constexpr unsigned WS_H = WS_O + (2u << 20);
constexpr unsigned WS_ST = WS_H + (2u << 20);
constexpr unsigned WS_M = WS_ST + (256u << 10);
constexpr unsigned WS_WGU = WS_M + (7u << 20);
constexpr unsigned WS_CS = WS_WGU + (3584u << 10);
constexpr unsigned WS_CNT = WS_CS + (448u << 10);
constexpr unsigned WS_BYTES = WS_CNT + 768 * 128;

typedef __amdgpu_buffer_rsrc_t rsrc_t;
typedef __attribute__((address_space(3))) const bf16x8* lds_fp;
typedef __attribute__((address_space(1))) unsigned gu32;
constexpr int SC1 = 16;
constexpr unsigned QKV_CODE_BYTES = 35840, MAIN_CODE_BYTES = 26112;

DI rsrc_t make_rsrc(const void* p, unsigned bytes) { return __builtin_amdgcn_make_buffer_rsrc((void*)p, 0, bytes, 0x00020000); }
DI void glds16(const void* g, void* l) { __builtin_amdgcn_global_load_lds((g_vp)g, (lds_vp)l, 16, 0, 0); }
DI void blds16(rsrc_t rs, unsigned voff, unsigned soff, char* l) { __builtin_amdgcn_raw_ptr_buffer_load_lds(rs, (lds_vp)l, 16, voff, soff, 0, 0); }
DI u32x4 ld16_dev(rsrc_t rs, unsigned voff, unsigned soff) { return __builtin_amdgcn_raw_buffer_load_b128(rs, voff, soff, SC1); }
DI void st16_dev(rsrc_t rs, unsigned voff, unsigned soff, u32x4 v) { __builtin_amdgcn_raw_buffer_store_b128(v, rs, voff + soff, 0, SC1); }
#define P1_AUXBITS 16
DI void st16_p1(rsrc_t rs, unsigned voff, unsigned soff, u32x4 v) { __builtin_amdgcn_raw_buffer_store_b128(v, rs, voff + soff, 0, P1_AUXBITS); }
DI bf16x8 lds_frag(const char* p) { return *(const bf16x8*)p; }
DI bf16x8 lds_frag_a(unsigned a) { return *(lds_fp)(size_t)a; }
DI unsigned lds_addr(const char* p) { return (unsigned)(size_t)(const __attribute__((address_space(3))) char*)p; }
DI unsigned touch(const char* g) { unsigned v; asm volatile("global_load_dword %0, %1, off" : "=v"(v) : "v"(g)); return v; }
#define KERNEL_ADDR(dst, sym) do { unsigned lo_, hi_; \
  asm volatile("s_getpc_b64 s[40:41]\n\ts_add_u32 s40, s40, " #sym "@rel32@lo+4\n\ts_addc_u32 s41, s41, " #sym "@rel32@hi+12\n\ts_mov_b32 %0, s40\n\ts_mov_b32 %1, s41" \
               : "=s"(lo_), "=s"(hi_) :: "s40", "s41", "scc"); \
  dst = ((unsigned long long)hi_ << 32) | lo_; } while (0)
DI void dma_piece(u32x4 rs, unsigned voff, unsigned soff, unsigned lds) {
  asm volatile("s_mov_b32 m0, %0\n\ts_nop 0\n\tbuffer_load_dwordx4 %1, %2, %3 offen lds" :: "s"(lds), "v"(voff), "s"(rs), "s"(soff));
}
DI unsigned opaque(unsigned v) { asm volatile("" : "+v"(v)); return v; }
DI bf16x8 tr_frag(const char* p0, const char* p1) {
  s16x4 lo = __builtin_amdgcn_ds_read_tr16_b64_v4i16((lds_s16x4)p0);
  s16x4 hi = __builtin_amdgcn_ds_read_tr16_b64_v4i16((lds_s16x4)p1);
  return __builtin_bit_cast(bf16x8, __builtin_shufflevector(lo, hi, 0, 1, 2, 3, 4, 5, 6, 7));
}
DI bf16x8 tr_frag_a(unsigned a0, unsigned a1) {
  s16x4 lo = __builtin_amdgcn_ds_read_tr16_b64_v4i16((lds_s16x4)(size_t)a0);
  s16x4 hi = __builtin_amdgcn_ds_read_tr16_b64_v4i16((lds_s16x4)(size_t)a1);
  return __builtin_bit_cast(bf16x8, __builtin_shufflevector(lo, hi, 0, 1, 2, 3, 4, 5, 6, 7));
}
DI bf16x8 pack8(float a0, float a1, float a2, float a3, float a4, float a5, float a6, float a7) {
  f32x8 v = {a0, a1, a2, a3, a4, a5, a6, a7};
  return __builtin_convertvector(v, bf16x8);
}
DI u32x4 as_u4(bf16x8 v) { return __builtin_bit_cast(u32x4, v); }
DI bf16x8 as_frag(u32x4 v) { return __builtin_bit_cast(bf16x8, v); }
DI float bf_lo(unsigned u) { return __uint_as_float(u << 16); }
DI float bf_hi(unsigned u) { return __uint_as_float(u & 0xffff0000u); }
DI float half_swap_sum(float v) {
  auto r = __builtin_amdgcn_permlane32_swap(__float_as_uint(v), __float_as_uint(v), false, false);
  return __uint_as_float(r[0]) + __uint_as_float(r[1]);
}
DI float half_swap_max(float v) {
  auto r = __builtin_amdgcn_permlane32_swap(__float_as_uint(v), __float_as_uint(v), false, false);
  return fmaxf(__uint_as_float(r[0]), __uint_as_float(r[1]));
}
DI void add_f32(float& a, float v) { asm("v_add_f32 %0, %0, %1" : "+v"(a) : "v"(v)); }
DI void fma_sq(float& a, float v) { asm("v_fma_f32 %0, %1, %1, %0" : "+v"(a) : "v"(v)); }
DI float colsum_frag(bf16x8 f, float acc) {
  const bf16x2 ones = {(__bf16)1.0f, (__bf16)1.0f};
  const u32x4 u = as_u4(f);
#pragma unroll
  for (int i = 0; i < 4; ++i) { const unsigned e = u[i]; acc = __builtin_amdgcn_fdot2_f32_bf16(__builtin_bit_cast(bf16x2, e), ones, acc, false); }
  return acc;
}

DI void signal_panel(char* ws, int phase, int panel) {
  if (threadIdx.x < 16)
    __hip_atomic_fetch_add((gu32*)(ws + WS_CNT + ((phase * 16 + panel) * 16 + threadIdx.x) * 128), 1u, __ATOMIC_RELAXED, __HIP_MEMORY_SCOPE_AGENT);
}
struct Polls { unsigned v[4]; };
DI void wait_panel(char* ws, int phase, int panel, int replica, int w, Polls& p) {
  const char* word = ws + WS_CNT + ((phase * 16 + panel) * 16 + replica) * 128;
  unsigned left = 1u << 17, seen;
  unsigned zero = 0;
  asm volatile(
      "s_cmp_lg_u32 %8, 0\n\ts_cbranch_scc1 2f\n"
      "1:\n\tglobal_load_dword %0, %6, %7 sc1\n\ts_waitcnt vmcnt(0)\n\tv_readfirstlane_b32 %5, %0\n\ts_cmp_ge_u32 %5, 16\n\ts_cbranch_scc1 2f\n\t" POLL_SLEEP
      "s_sub_u32 %4, %4, 1\n\ts_cmp_lg_u32 %4, 0\n\ts_cbranch_scc1 1b\n"
      "2:"
      : "=&v"(p.v[0]), "=&v"(p.v[1]), "=&v"(p.v[2]), "=&v"(p.v[3]), "+s"(left), "=&s"(seen)
      : "v"(zero), "s"(word), "s"(w)
      : "scc", "memory");
  return;
#define POLL_STEP(k) "s_waitcnt vmcnt(3)\n\tv_readfirstlane_b32 %5, %" #k "\n\ts_cmp_ge_u32 %5, 16\n\ts_cbranch_scc1 2f\n\tglobal_load_dword %" #k ", %6, %7 sc1\n\t"
  asm volatile(
      "s_cmp_lg_u32 %8, 0\n\ts_cbranch_scc1 2f\n\t"
      "global_load_dword %0, %6, %7 sc1\n\ts_sleep " POLL_GAP "\n\t"
      "global_load_dword %1, %6, %7 sc1\n\ts_sleep " POLL_GAP "\n\t"
      "global_load_dword %2, %6, %7 sc1\n\ts_sleep " POLL_GAP "\n\t"
      "global_load_dword %3, %6, %7 sc1\n"
      "1:\n\t" POLL_STEP(0) POLL_STEP(1) POLL_STEP(2) POLL_STEP(3)
      "s_sub_u32 %4, %4, 1\n\ts_cmp_lg_u32 %4, 0\n\ts_cbranch_scc1 1b\n"
      "2:"
      : "=&v"(p.v[0]), "=&v"(p.v[1]), "=&v"(p.v[2]), "=&v"(p.v[3]), "+s"(left), "=&s"(seen)
      : "v"(zero), "s"(word), "s"(w)
      : "scc", "memory");
#undef POLL_STEP
}
DI void retire(const Polls& p) { asm volatile("; retire %0 %1 %2 %3" :: "v"(p.v[0]), "v"(p.v[1]), "v"(p.v[2]), "v"(p.v[3])); }
constexpr int P1_SLOT = 28672, P1_NSLOT = 5, P1_AUX = P1_SLOT * P1_NSLOT, P1_PREP = P1_AUX + 4096;
constexpr int P1_LDS = P1_PREP + 16384;

template <int VMASK>
DI void p1_compute(char* smem, int w, int lane, int p, int j, const char* wqkv, char* ws) {
  const int r = lane & 31, h = lane >> 5;
  const int row = 32 * w + r, sw = (r >> 1) & 7;
  const unsigned lb = lds_addr(smem);
  unsigned xa[4][3], wa[3];
#pragma unroll
  for (int ks = 0; ks < 4; ++ks) {
    const unsigned o = lb + row * 128 + (((2 * ks + h) ^ sw) << 4);
#pragma unroll
    for (int b = 0; b < 3; ++b) xa[ks][b] = opaque(o + b * 2 * P1_SLOT);
  }
  {
    const int q = (lane & 15) >> 2, pp = lane & 3, blk = (lane >> 4) & 1;
    const unsigned o = lb + 16384 + (8 * h + q) * 192 + (2 * blk + (pp >> 1)) * 16 + 8 * (pp & 1);
#pragma unroll
    for (int b = 0; b < 3; ++b) wa[b] = opaque(o + b * 2 * P1_SLOT);
  }
  f32x16 acc[3] = {};
  float cs[3] = {0.f, 0.f, 0.f};
  bf16x8 xf[3], wf[3][3];
  auto rdx = [&](int g) { const int sl = (g >> 2) % P1_NSLOT; xf[g % 3] = lds_frag_a(xa[g & 3][sl >> 1] + (sl & 1) * P1_SLOT); };
  auto rdw = [&](int g, int c) {
    const int sl = (g >> 2) % P1_NSLOT;
    const unsigned a = wa[sl >> 1] + (sl & 1) * P1_SLOT + (16 * (g & 3)) * 192 + c * 64;
    wf[g % 3][c] = tr_frag_a(a, a + 4 * 192);
  };
  auto mm = [&](int g, int c) {
    if ((VMASK >> c) & 1) acc[c] = MFMA32(xf[g % 3], wf[g % 3][c], acc[c]);
    else acc[c] = MFMA32(wf[g % 3][c], xf[g % 3], acc[c]);
  };
  const unsigned long long wa64 = (unsigned long long)wqkv;
  const u32x4 rw = {(unsigned)wa64, (unsigned)(wa64 >> 32) & 0xffffu, (unsigned)(D * 3 * D * 2), 0x00020000u};
  unsigned wo_[3];
#pragma unroll
  for (int i = 0; i < 3; ++i) { const int g = 64 * (3 * w + i) + lane; wo_[i] = (g / 12) * 3072 + 192 * j + (g % 12) * 16; }
  auto wpiece = [&](int s, int i) { dma_piece(rw, wo_[i], s * 64 * 3072, lb + (s % P1_NSLOT) * P1_SLOT + 16384 + (3 * w + i) * 1024); };
#pragma unroll
  for (int s = 0; s < 3; ++s) { wpiece(s, 0); wpiece(s, 1); wpiece(s, 2); }
  unsigned warm;
  {
    unsigned long long ka, kb;
    KERNEL_ADDR(ka, k_qkv); KERNEL_ADDR(kb, k_main);
    const unsigned i = blockIdx.x >> 3, v = 4 * w + (lane & 3);
    const char* src = v < 9 ? (const char*)ka + min((i * 9 + v) * 128u, QKV_CODE_BYTES - 128u) : (const char*)kb + min((i * 7 + v - 9) * 128u, MAIN_CODE_BYTES - 128u);
    asm volatile("global_load_dword %0, %1, off" : "=v"(warm) : "v"(src));
  }
  WAIT_VM(7); BARRIER();
#pragma unroll
  for (int g = 0; g < 2; ++g) { rdx(g); rdw(g, 0); rdw(g, 1); rdw(g, 2); }
  SCHED_FENCE();
#pragma unroll
  for (int g = 0; g < 32; ++g) {
    const int n = g + 2;
    mm(g, 0); SCHED_FENCE();
    if (n < 32) {
      if ((n & 3) == 0) {
        if (n <= 8) WAIT_VM(7); else if (n <= 20) WAIT_VM(6); else if (n == 24) WAIT_VM(3); else WAIT_VM(0);
        WAIT_LGKM0(); BARRIER();
      }
      rdx(n); rdw(n, 0); SCHED_FENCE();
    }
    mm(g, 1); SCHED_FENCE();
    {
      const int m = g < 2 ? g : g + 2;
      const int s = g < 2 ? 3 : (m >> 2) + 3, i = g < 2 ? g : (m & 3);
      if (s < 8 && i < 3) { wpiece(s, i); SCHED_FENCE(); }
      if (g == 1) { wpiece(3, 2); SCHED_FENCE(); }
    }
    if (n < 32) { rdw(n, 1); rdw(n, 2); SCHED_FENCE(); }
    mm(g, 2); SCHED_FENCE();
    if ((g & 3) == w) {
#pragma unroll
      for (int c = 0; c < 3; ++c) cs[c] = colsum_frag(wf[g % 3][c], cs[c]);
    }
    SCHED_FENCE();
  }
  WAIT_VM(0);
  asm volatile("; warm %0" :: "v"(warm));
  float* csp = (float*)(smem + P1_AUX);
  float* cst = csp + 384;
  f32x2* st = (f32x2*)(smem + P1_AUX + 2048);
#pragma unroll
  for (int c = 0; c < 3; ++c) { float t = half_swap_sum(cs[c]); if (h == 0) csp[w * 96 + c * 32 + r] = t; }
  WAIT_LGKM0(); BARRIER();
  if (w < 2) { int t = w * 64 + lane; if (t < 96) cst[t] = csp[t] + csp[96 + t] + csp[192 + t] + csp[288 + t]; }
  WAIT_LGKM0(); BARRIER();
  const int tb = 4 * p + w;
  const rsrc_t rws = make_rsrc(ws, WS_BYTES);
  const f32x2 mine = st[row];
#pragma unroll
  for (int c = 0; c < 3; ++c) {
    const int c0 = 96 * j + 32 * c;
    if ((VMASK >> c) & 1) {
      const float csv = cst[32 * c + r];
      float v[16];
#pragma unroll
      for (int i = 0; i < 16; ++i) { f32x2 t = st[32 * w + (i & 3) + 8 * (i >> 2) + 4 * h]; v[i] = acc[c][i] * t[0] - t[1] * csv; }
      const unsigned dst = WS_V + (((c0 - 1024) >> 5) * 128 + 2 * tb) * 1024;
      st16_p1(rws, lane * 16, dst, as_u4(pack8(v[0], v[1], v[2], v[3], v[4], v[5], v[6], v[7])));
      st16_p1(rws, lane * 16, dst + 1024, as_u4(pack8(v[8], v[9], v[10], v[11], v[12], v[13], v[14], v[15])));
    } else {
      const bool isq = c0 < 512;
      const float sc = isq ? Q_SCALE : 1.0f;
      const float a = mine[0] * sc, b = mine[1] * sc;
      float v[16];
#pragma unroll
      for (int g = 0; g < 4; ++g) {
        f32x4 cv = *(const f32x4*)(cst + 32 * c + 8 * g + 4 * h);
#pragma unroll
        for (int i = 0; i < 4; ++i) v[4 * g + i] = acc[c][4 * g + i] * a - b * cv[i];
      }
      const int head = (c0 & 511) >> 4;
      const unsigned dst = (isq ? WS_Q : WS_K) + (head * 64 + tb) * 1024;
      st16_p1(rws, lane * 16, dst, as_u4(pack8(v[0], v[1], v[2], v[3], v[4], v[5], v[6], v[7])));
      st16_p1(rws, lane * 16, dst + 65536, as_u4(pack8(v[8], v[9], v[10], v[11], v[12], v[13], v[14], v[15])));
    }
  }
}

DI void p1_helper(char* smem, int lw, int lane, int p, int j, const char* x, const char* wg, const char* wu, char* ws) {
  const rsrc_t rx = make_rsrc(x, S * D * 2);
  unsigned xo[4];
#pragma unroll
  for (int i = 0; i < 4; ++i) {
    int row = 32 * lw + 8 * i + (lane >> 3);
    xo[i] = (128 * p + row) * 1024 + (((lane & 7) ^ ((row >> 1) & 7)) << 4);
  }
  auto issue = [&](int s) {
    char* sl = smem + (s % P1_NSLOT) * P1_SLOT;
#pragma unroll
    for (int i = 0; i < 4; ++i) blds16(rx, xo[i], s * 128, sl + (4 * lw + i) * 1024);
  };
  issue(0); issue(1); issue(2);
  const int b = blockIdx.x, cg = b % 28, pks = 4 * (b / 28) + lw;
  const bool prep = b < 224;
  char* stg = smem + P1_PREP + lw * 4096;
  auto prep_issue = [&]() {
    const rsrc_t rg = make_rsrc(wg, D * F * 2), ru = make_rsrc(wu, D * F * 2);
    const unsigned off = (16 * pks + (lane >> 3)) * 3584 + cg * 128 + (lane & 7) * 16;
    blds16(rg, off, 0, stg); blds16(rg, off, 8 * 3584, stg + 1024);
    blds16(ru, off, 0, stg + 2048); blds16(ru, off, 8 * 3584, stg + 3072);
  };
  const int r = lane & 31, h = lane >> 5, row = 32 * lw + r, sw = (r >> 1) & 7;
  bf16x8 pf_[4];
  auto prep_read = [&]() {
    const int blk = (lane >> 4) & 1, q = (lane & 15) >> 2, pp = lane & 3;
    const unsigned bs = lds_addr(stg) + blk * 2048 + (4 * h + q) * 128 + (pp >> 1) * 16 + 8 * (pp & 1);
    u32x2 t[8];
#define PREP_RD(i, off) asm volatile("ds_read_b64_tr_b16 %0, %1 offset:" #off : "=v"(t[i]) : "v"(bs))
    PREP_RD(0, 0); PREP_RD(1, 1024); PREP_RD(2, 32); PREP_RD(3, 1056); PREP_RD(4, 64); PREP_RD(5, 1088); PREP_RD(6, 96); PREP_RD(7, 1120);
#undef PREP_RD
    asm volatile("s_waitcnt lgkmcnt(0)" : "+v"(t[0]), "+v"(t[1]), "+v"(t[2]), "+v"(t[3]), "+v"(t[4]), "+v"(t[5]), "+v"(t[6]), "+v"(t[7]));
#pragma unroll
    for (int i = 0; i < 4; ++i) { const u32x2 lo = t[2 * i], hi = t[2 * i + 1]; pf_[i] = as_frag(u32x4{lo[0], lo[1], hi[0], hi[1]}); }
  };
  auto prep_frag = [&](int i) {
    const int fs = 4 * cg + i, jj = fs / 7, t = fs % 7;
    st16_p1(make_rsrc(ws, WS_BYTES), lane * 16, WS_WGU + ((jj * 32 + pks) * 7 + t) * 1024, as_u4(pf_[i]));
    const float c = half_swap_sum(colsum_frag(pf_[i], 0.f));
    if (h == 0) *(float*)(ws + WS_CS + ((size_t)pks * 112 + fs) * 128 + r * 4) = c;
  };
  float sum0 = 0.f, sum1 = 0.f, sq0 = 0.f, sq1 = 0.f;
  auto piece = [&](int s, int i) { blds16(rx, xo[i], s * 128, smem + (s % P1_NSLOT) * P1_SLOT + (4 * lw + i) * 1024); };
  auto step = [&](int s, int n) {
    const char* sl = smem + (s % P1_NSLOT) * P1_SLOT + row * 128;
    u32x4 u[4];
#pragma unroll
    for (int c = 0; c < 4; ++c) u[c] = *(const u32x4*)(sl + (((4 * h + c) ^ sw) << 4));
    SCHED_FENCE();
#pragma unroll
    for (int c = 0; c < 4; ++c) {
      if (n < 8) piece(n, c);
      SCHED_FENCE();
#pragma unroll
      for (int i = 0; i < 4; ++i) {
        const float lo = bf_lo(u[c][i]), hi = bf_hi(u[c][i]);
        add_f32(sum0, lo); add_f32(sum1, hi); fma_sq(sq0, lo); fma_sq(sq1, hi);
      }
      SCHED_FENCE();
    }
  };
  if (prep) {
    prep_issue();
    WAIT_VM(12); BARRIER();
    step(0, 3); WAIT_VM(12); WAIT_LGKM0(); BARRIER();
    step(1, 4); WAIT_VM(12); WAIT_LGKM0(); BARRIER();
  } else {
    WAIT_VM(8); BARRIER();
    step(0, 3); WAIT_VM(8); WAIT_LGKM0(); BARRIER();
    step(1, 4); WAIT_VM(8); WAIT_LGKM0(); BARRIER();
  }
  step(2, 5); WAIT_VM(8); WAIT_LGKM0(); BARRIER();
  step(3, 6); WAIT_VM(8); WAIT_LGKM0(); BARRIER();
  step(4, 7); WAIT_VM(8); WAIT_LGKM0(); BARRIER();
  step(5, 8); WAIT_VM(4);
  if (prep) prep_read();
  WAIT_LGKM0(); BARRIER();
  step(6, 8); WAIT_VM(0);
  if (prep) prep_frag(0);
  WAIT_LGKM0(); BARRIER();
  step(7, 8);
  const float s1 = half_swap_sum(sum0 + sum1), s2 = half_swap_sum(sq0 + sq1);
  const float mean = s1 * (1.0f / D);
  const float var = fmaxf(s2 * (1.0f / D) - mean * mean, 0.f);
  const float rstd = 1.0f / sqrtf(var + LN_EPS);
  f32x2* st = (f32x2*)(smem + P1_AUX + 2048);
  if (h == 0) st[row] = f32x2{rstd, mean * rstd};
  WAIT_LGKM0(); BARRIER();
  BARRIER();
  if (prep) prep_frag(1);
  if (prep) { prep_frag(2); prep_frag(3); }
}

extern "C" __global__ __launch_bounds__(512, 2) void k_qkv(const char* __restrict__ x, const char* __restrict__ wqkv, const char* __restrict__ wg,
                                                const char* __restrict__ wu, char* __restrict__ ws) {
  extern __shared__ __attribute__((aligned(16))) char smem[];
  const int w = __builtin_amdgcn_readfirstlane(threadIdx.x >> 6), lane = threadIdx.x & 63;
  const int xcd = blockIdx.x & 7, i = blockIdx.x >> 3;
  const int p = 4 * (xcd & 3) + (i & 3), j = 8 * (xcd >> 2) + (i >> 2);
  if (threadIdx.x < 3) *(unsigned*)(ws + WS_CNT + (blockIdx.x * 3 + threadIdx.x) * 128) = 0u;
  if (w >= 4) p1_helper(smem, w - 4, lane, p, j, x, wg, wu, ws);
  else if (j <= 9) p1_compute<0>(smem, w, lane, p, j, wqkv, ws);
  else if (j == 10) p1_compute<4>(smem, w, lane, p, j, wqkv, ws);
  else p1_compute<7>(smem, w, lane, p, j, wqkv, ws);
}

constexpr int P2_K = 0, P2_V = 24576, P2_VP = 1040, P2_Q = P2_V + 24 * P2_VP;
constexpr int L_WO = 77824;
constexpr int L_RED = 143360;
constexpr int L_CS = L_RED + 16384;
constexpr int MAIN_LDS = 163840;

template <bool FULL>
DI void attn_task(const char* smem, int lane, int hh, int wq, int qb, rsrc_t rws, int head) {
  const int r = lane & 31, h = lane >> 5;
  const char* kb_ = smem + P2_K + (hh * 12 + wq) * 1024 + lane * 16;
  const bool valid = (r >> 4) == hh;
  const char* vb_ = smem + P2_V + 2 * wq * P2_VP + (valid ? lane * 16 : 1024);
  const bf16x8 qf = lds_frag(smem + P2_Q + (hh * 4 + wq) * 1024 + lane * 16);
  const f32x16 zero = {};
  const int t0 = FULL ? 1 : ((8 - qb) > 1 ? (8 - qb) : 1);
  const bool have_first = FULL || qb >= 8;
  f32x16 sm;
  {
    const f32x16 sd = MFMA32(lds_frag(kb_ + 8 * 1024), qf, zero);
    f32x16 sf;
    if (have_first) sf = MFMA32(lds_frag(kb_), qf, zero);
    else { for (int i = 0; i < 16; ++i) sf[i] = -1e30f; }
#pragma unroll
    for (int i = 0; i < 16; ++i) { const int jj = (i & 3) + 8 * (i >> 2) + 4 * h; sm[i] = jj <= r ? sd[i] : sf[i]; }
  }
  float m = sm[0];
#pragma unroll
  for (int i = 1; i < 16; ++i) m = fmaxf(m, sm[i]);
  if (FULL) {
#pragma unroll
    for (int t = 1; t < 8; ++t) {
      const f32x16 s = MFMA32(lds_frag(kb_ + t * 1024), qf, zero);
#pragma unroll
      for (int i = 0; i < 16; i += 2) m = fmaxf(fmaxf(m, s[i]), s[i + 1]);
    }
  } else {
    for (int t = t0; t < 8; ++t) {
      const f32x16 s = MFMA32(lds_frag(kb_ + t * 1024), qf, zero);
#pragma unroll
      for (int i = 0; i < 16; i += 2) m = fmaxf(fmaxf(m, s[i]), s[i + 1]);
    }
  }
  m = half_swap_max(m);
  f32x16 negm;
#pragma unroll
  for (int i = 0; i < 16; ++i) negm[i] = -m;
  f32x16 o = zero;
  {
    float pv[16];
#pragma unroll
    for (int i = 0; i < 16; ++i) pv[i] = __builtin_amdgcn_exp2f(sm[i] - m);
    u32x4 pa = __builtin_bit_cast(u32x4, pack8(pv[0], pv[1], pv[2], pv[3], pv[4], pv[5], pv[6], pv[7]));
    u32x4 pb = __builtin_bit_cast(u32x4, pack8(pv[8], pv[9], pv[10], pv[11], pv[12], pv[13], pv[14], pv[15]));
    u32x4 da, db;
#pragma unroll
    for (int i = 0; i < 4; ++i) {
      const int ja = ((2 * i) & 3) + 8 * ((2 * i) >> 2) + 4 * h;
      const unsigned ma = (ja <= r ? 0xffffu : 0u) | (ja + 1 <= r ? 0xffff0000u : 0u);
      const unsigned mb = (ja + 16 <= r ? 0xffffu : 0u) | (ja + 17 <= r ? 0xffff0000u : 0u);
      da[i] = pa[i] & ma; db[i] = pb[i] & mb;
    }
    o = MFMA32(lds_frag(vb_ + 16 * P2_VP), __builtin_bit_cast(bf16x8, da), o);
    o = MFMA32(lds_frag(vb_ + 17 * P2_VP), __builtin_bit_cast(bf16x8, db), o);
    if (have_first) {
      o = MFMA32(lds_frag(vb_), __builtin_bit_cast(bf16x8, pa ^ da), o);
      o = MFMA32(lds_frag(vb_ + P2_VP), __builtin_bit_cast(bf16x8, pb ^ db), o);
    }
  }
  auto tile = [&](int t) {
    const f32x16 s = MFMA32(lds_frag(kb_ + t * 1024), qf, negm);
    float pv[16];
#pragma unroll
    for (int i = 0; i < 16; ++i) pv[i] = __builtin_amdgcn_exp2f(s[i]);
    o = MFMA32(lds_frag(vb_ + (2 * t) * P2_VP), pack8(pv[0], pv[1], pv[2], pv[3], pv[4], pv[5], pv[6], pv[7]), o);
    o = MFMA32(lds_frag(vb_ + (2 * t + 1) * P2_VP), pack8(pv[8], pv[9], pv[10], pv[11], pv[12], pv[13], pv[14], pv[15]), o);
  };
  if (FULL) {
#pragma unroll
    for (int t = 1; t < 8; ++t) tile(t);
  } else {
    for (int t = t0; t < 8; ++t) tile(t);
  }
  const float l = hh ? o[0] : o[8];
  const float inv = 1.0f / l;
  float ov[8];
#pragma unroll
  for (int i = 0; i < 8; ++i) ov[i] = (hh ? o[8 + i] : o[i]) * inv;
  st16_dev(rws, lane * 16, WS_O + (((qb >> 1) * 32 + head) * 2 + (qb & 1)) * 1024, as_u4(pack8(ov[0], ov[1], ov[2], ov[3], ov[4], ov[5], ov[6], ov[7])));
}

DI unsigned wrow_voff(int lane) { return (lane >> 3) * 1024 + (((lane & 7) ^ (((lane >> 4) & 1) << 2)) << 4); }
DI unsigned wtr_off(int lane, int nt) {
  const int h = lane >> 5, blk = (lane >> 4) & 1, q = (lane & 15) >> 2, pp = lane & 3, sw = (q >> 1) & 1;
  return (4 * h + q) * 128 + (2 * blk + (pp >> 1) + 4 * (nt ^ sw)) * 16 + 8 * (pp & 1);
}

DI void phase_attn(char* smem, int w, int lane, char* ws, rsrc_t rws, const char* wo) {
  const int xcd = blockIdx.x & 7, i = blockIdx.x >> 3;
  const int hp = 2 * xcd + (i & 1), p = i >> 1;
  const int kb0 = 4 * p - 8;
  const int hh = w >> 2, wq = w & 3, qb = 4 * p + wq, head = 2 * hp + hh;
  blds16(rws, lane * 16, WS_Q + (head * 64 + qb) * 1024, smem + P2_Q + w * 1024);
#pragma unroll
  for (int n = 0; n < 3; ++n) {
    const int t = wq * 3 + n, sv = w * 3 + n;
    const int kt = kb0 + t < 0 ? 0 : kb0 + t, vt = 2 * kb0 + sv < 0 ? 0 : 2 * kb0 + sv;
    blds16(rws, lane * 16, WS_K + (head * 64 + kt) * 1024, smem + P2_K + (hh * 12 + t) * 1024);
    blds16(rws, lane * 16, WS_V + (hp * 128 + vt) * 1024, smem + P2_V + sv * P2_VP);
  }
  if (threadIdx.x < 24) *(u32x4*)(smem + P2_V + threadIdx.x * P2_VP + 1024) = u32x4{0x3f803f80u, 0x3f803f80u, 0x3f803f80u, 0x3f803f80u};
  WAIT_VM(0); WAIT_LGKM0(); BARRIER();
  {
    const rsrc_t rwo = make_rsrc(wo, D * D * 2);
    const int c = i >> 2;
    const unsigned vo = wrow_voff(lane);
#pragma unroll
    for (int n = 0; n < 8; ++n) blds16(rwo, vo, (8 * w + n) * 8192 + c * 128, smem + L_WO + (8 * w + n) * 1024);
  }
  if (qb >= 8) attn_task<true>(smem, lane, hh, wq, qb, rws, head);
  else attn_task<false>(smem, lane, hh, wq, qb, rws, head);
  WAIT_VM(0); WAIT_LGKM0(); BARRIER();
  signal_panel(ws, 0, p);
}

template <int NACC>
DI f32x16 pair_reduce(char* smem, int w, int lane, const f32x16 (&acc)[NACC]) {
  const int kh = w >> 1;
  const f32x16 send = kh ? acc[0] : acc[1];
  f32x16 a = kh ? acc[1] : acc[0];
#pragma unroll
  for (int g = 0; g < 4; ++g)
    *(f32x4*)(smem + L_RED + (w * 4 + g) * 1024 + lane * 16) = f32x4{send[4 * g], send[4 * g + 1], send[4 * g + 2], send[4 * g + 3]};
  WAIT_LGKM0(); BARRIER();
#pragma unroll
  for (int g = 0; g < 4; ++g) {
    const f32x4 v = *(const f32x4*)(smem + L_RED + ((w ^ 2) * 4 + g) * 1024 + lane * 16);
    a[4 * g] += v[0]; a[4 * g + 1] += v[1]; a[4 * g + 2] += v[2]; a[4 * g + 3] += v[3];
  }
  return a;
}

DI void oproj_compute(char* smem, int w, int lane, char* ws, rsrc_t rws, const char* x, int tp, int c) {
  const int r = lane & 31, h = lane >> 5, tt = w & 1, kh = w >> 1;
  const int tb = 2 * tp + tt, ntile = 2 * c + kh;
  u32x2 rx[4];
#pragma unroll
  for (int g = 0; g < 4; ++g) rx[g] = *(const u32x2*)(x + (size_t)(32 * tb + r) * 1024 + (32 * ntile + 8 * g + 4 * h) * 2);
  Polls pl;
  wait_panel(ws, 0, tp >> 1, (tp & 1) * 8 + c, w, pl);
  BARRIER();
  u32x4 xf[16];
#pragma unroll
  for (int g = 0; g < 16; ++g) xf[g] = ld16_dev(rws, lane * 16, WS_O + ((tp * 32 + 16 * kh + g) * 2 + tt) * 1024);
  const unsigned lb = lds_addr(smem) + L_WO + kh * 32768;
  const unsigned a0 = opaque(lb + wtr_off(lane, 0)), a1 = opaque(lb + wtr_off(lane, 1));
  bf16x8 wf[3][2];
  auto rd = [&](int g) {
    wf[g % 3][0] = tr_frag_a(a0 + g * 2048, a0 + g * 2048 + 1024);
    wf[g % 3][1] = tr_frag_a(a1 + g * 2048, a1 + g * 2048 + 1024);
  };
  f32x16 acc[2] = {};
  rd(0); rd(1);
  SCHED_FENCE();
#pragma unroll
  for (int g = 0; g < 16; ++g) {
    acc[0] = MFMA32(wf[g % 3][0], as_frag(xf[g]), acc[0]); SCHED_FENCE();
    if (g == 0) retire(pl);
    if (g + 2 < 16) { rd(g + 2); SCHED_FENCE(); }
    acc[1] = MFMA32(wf[g % 3][1], as_frag(xf[g]), acc[1]); SCHED_FENCE();
  }
  f32x16 a = pair_reduce<2>(smem, w, lane, acc);
#pragma unroll
  for (int g = 0; g < 4; ++g) {
    a[4 * g] += bf_lo(rx[g][0]); a[4 * g + 1] += bf_hi(rx[g][0]); a[4 * g + 2] += bf_lo(rx[g][1]); a[4 * g + 3] += bf_hi(rx[g][1]);
  }
  float s1 = 0.f, s2 = 0.f;
#pragma unroll
  for (int s = 0; s < 2; ++s) {
    const u32x4 u = as_u4(pack8(a[8 * s], a[8 * s + 1], a[8 * s + 2], a[8 * s + 3], a[8 * s + 4], a[8 * s + 5], a[8 * s + 6], a[8 * s + 7]));
    st16_dev(rws, lane * 16, WS_H + (tb * 32 + 2 * ntile + s) * 1024, u);
#pragma unroll
    for (int i = 0; i < 4; ++i) { const float lo = bf_lo(u[i]), hi = bf_hi(u[i]); s1 += lo + hi; s2 += lo * lo + hi * hi; }
  }
  s1 = half_swap_sum(s1); s2 = half_swap_sum(s2);
  if (h == 0) __builtin_amdgcn_raw_buffer_store_b64(u32x2{__float_as_uint(s1), __float_as_uint(s2)}, rws, r * 8, WS_ST + (tb * 16 + ntile) * 256, SC1);
  WAIT_VM(0); BARRIER();
  signal_panel(ws, 1, tp >> 1);
}

#define XD4 12
constexpr int P4_SLOT = 28672, P4_XD = XD4;

DI void gateup_compute(char* smem, int w, int lane, char* ws, rsrc_t rws, int p, int j) {
  const int r = lane & 31, h = lane >> 5, tb = 4 * p + w;
  Polls pl;
  wait_panel(ws, 1, p, j, w, pl);
  BARRIER();
  u32x4 xf[32];
  auto ldx = [&](int g) { xf[g] = ld16_dev(rws, lane * 16, WS_H + (tb * 32 + g) * 1024); };
  ldx(0); ldx(1);
  u32x2 sv[8];
#pragma unroll
  for (int i = 0; i < 8; ++i) sv[i] = __builtin_amdgcn_raw_buffer_load_b64(rws, (8 * h + i) * 256 + r * 8, WS_ST + tb * 4096, SC1);
#pragma unroll
  for (int g = 2; g < P4_XD; ++g) ldx(g);
  const unsigned lb = lds_addr(smem) + lane * 16;
  unsigned ba[3];
#pragma unroll
  for (int b = 0; b < 3; ++b) ba[b] = opaque(lb + b * 2 * P4_SLOT);
  bf16x8 wf[2][7];
  auto rdw = [&](int g, int t) {
    const int sl = (g >> 2) % 5;
    wf[g & 1][t] = lds_frag_a(ba[sl >> 1] + (sl & 1) * P4_SLOT + ((g & 3) * 7 + t) * 1024);
  };
  f32x16 acc[7] = {};
#pragma unroll
  for (int t = 0; t < 7; ++t) rdw(0, t);
  float s1 = 0.f, s2 = 0.f;
  SCHED_FENCE();
#pragma unroll
  for (int g = 0; g < 32; ++g) {
    const int n = g + 1;
#pragma unroll
    for (int t = 0; t < 7; ++t) {
      acc[t] = MFMA32(wf[g & 1][t], as_frag(xf[g]), acc[t]); SCHED_FENCE();
      if (g == 0 && t == 0) retire(pl);
      if (t == 0 && n < 32 && (n & 3) == 0) { WAIT_LGKM0(); BARRIER(); }
      if (t == 0 && n == 32) { WAIT_LGKM0(); BARRIER(); }
      if (n < 32) rdw(n, t);
      if (t == 1 && g + P4_XD < 32) ldx(g + P4_XD);
      if (g == 2 && t == 2) {
#pragma unroll
        for (int i = 0; i < 8; ++i) { const unsigned e0 = sv[i][0], e1 = sv[i][1]; s1 += __uint_as_float(e0); s2 += __uint_as_float(e1); }
      }
      SCHED_FENCE();
    }
  }
  s1 = half_swap_sum(s1); s2 = half_swap_sum(s2);
  const float mean = s1 * (1.0f / D);
  const float rstd = 1.0f / sqrtf(fmaxf(s2 * (1.0f / D) - mean * mean, 0.f) + LN_EPS);
  const float mr = mean * rstd;
  const float* cst = (const float*)(smem + L_CS);
  const unsigned dst = WS_M + (((tb >> 1) * 112 + 7 * j) * 2 + (tb & 1)) * 1024;
#pragma unroll
  for (int t = 0; t < 7; ++t) {
    float v[8];
#pragma unroll
    for (int g = 0; g < 2; ++g) {
      const f32x4 cg = *(const f32x4*)(cst + 32 * t + 8 * g + 4 * h), cu = *(const f32x4*)(cst + 32 * t + 16 + 8 * g + 4 * h);
#pragma unroll
      for (int i = 0; i < 4; ++i) {
        const float gv = acc[t][4 * g + i] * rstd - mr * cg[i], uv = acc[t][8 + 4 * g + i] * rstd - mr * cu[i];
        v[4 * g + i] = fmaxf(gv, 0.f) * uv;
      }
    }
    st16_dev(rws, lane * 16, dst + t * 2048, as_u4(pack8(v[0], v[1], v[2], v[3], v[4], v[5], v[6], v[7])));
  }
  WAIT_VM(0); BARRIER();
  signal_panel(ws, 2, p);
}

#define XD5 8
constexpr int P5_SLOT = 32768, P5_XD = XD5;
DI constexpr int p5_slot(int it) { return ((it + 3) & 3) * P5_SLOT; }

DI int p5_rot(int it, int rot) { const int v = it + rot; return v >= 7 ? v - 7 : v; }

DI void down_compute(char* smem, int w, int lane, char* ws, rsrc_t rws, char* out, int tp, int c, int rot) {
  const int r = lane & 31, h = lane >> 5, tt = w & 1, kh = w >> 1;
  const int tb = 2 * tp + tt, ntile = 2 * c + kh;
  u32x4 rh[2];
#pragma unroll
  for (int s = 0; s < 2; ++s) rh[s] = ld16_dev(rws, lane * 16, WS_H + (tb * 32 + 2 * ntile + s) * 1024);
  Polls pl;
  wait_panel(ws, 2, tp >> 1, (tp & 1) * 8 + c, w, pl);
  BARRIER();
  u32x4 xf[56];
  unsigned mb[7];
#pragma unroll
  for (int it = 0; it < 7; ++it) mb[it] = WS_M + ((tp * 112 + 56 * kh + 8 * p5_rot(it, rot)) * 2 + tt) * 1024;
  auto ldx = [&](int g) { xf[g] = ld16_dev(rws, lane * 16, mb[g >> 3] + (g & 7) * 2048); };
#pragma unroll
  for (int g = 0; g < P5_XD; ++g) ldx(g);
  const unsigned lb = lds_addr(smem) + kh * 16384;
  unsigned a0[2], a1[2];
#pragma unroll
  for (int b = 0; b < 2; ++b) { a0[b] = opaque(lb + wtr_off(lane, 0) + b * 65536); a1[b] = opaque(lb + wtr_off(lane, 1) + b * 65536); }
  bf16x8 wf[3][2];
  auto rd = [&](int g) {
    const int o = p5_slot(g >> 3) + (g & 7) * 2048, b = o >> 16, oo = o & 65535;
    wf[g % 3][0] = tr_frag_a(a0[b] + oo, a0[b] + oo + 1024);
    wf[g % 3][1] = tr_frag_a(a1[b] + oo, a1[b] + oo + 1024);
  };
  f32x16 acc[2] = {};
  rd(0); rd(1);
  SCHED_FENCE();
#pragma unroll
  for (int g = 0; g < 56; ++g) {
    const int n = g + 2;
    acc[0] = MFMA32(wf[g % 3][0], as_frag(xf[g]), acc[0]); SCHED_FENCE();
    if (g == 0) retire(pl);
    if (n < 56) {
      if ((n & 7) == 0) { WAIT_LGKM0(); BARRIER(); }
      rd(n);
    }
    if (g + P5_XD < 56) ldx(g + P5_XD);
    SCHED_FENCE();
    acc[1] = MFMA32(wf[g % 3][1], as_frag(xf[g]), acc[1]); SCHED_FENCE();
  }
  f32x16 a = pair_reduce<2>(smem, w, lane, acc);
  unsigned pk[8];
#pragma unroll
  for (int s = 0; s < 2; ++s) {
#pragma unroll
    for (int i = 0; i < 4; ++i) { a[8 * s + 2 * i] += bf_lo(rh[s][i]); a[8 * s + 2 * i + 1] += bf_hi(rh[s][i]); }
    const u32x4 u = as_u4(pack8(a[8 * s], a[8 * s + 1], a[8 * s + 2], a[8 * s + 3], a[8 * s + 4], a[8 * s + 5], a[8 * s + 6], a[8 * s + 7]));
#pragma unroll
    for (int i = 0; i < 4; ++i) pk[4 * s + i] = u[i];
  }
  char* dst = out + (size_t)(32 * tb + r) * 1024 + (32 * ntile + 8 * h) * 2;
#pragma unroll
  for (int s = 0; s < 2; ++s) {
    auto q0 = __builtin_amdgcn_permlane32_swap(pk[4 * s], pk[4 * s + 2], false, false);
    auto q1 = __builtin_amdgcn_permlane32_swap(pk[4 * s + 1], pk[4 * s + 3], false, false);
    *(u32x4*)(dst + s * 32) = u32x4{q0[0], q1[0], q0[1], q1[1]};
  }
}

DI void loader_345(char* smem, int lw, int lane, char* ws, rsrc_t rws, const char* wd, int j, int c, int rot) {
  f32x4 cv[8];
  const int cl = lane < 56 ? lane : 55;
#pragma unroll
  for (int i = 0; i < 8; ++i) cv[i] = *(const f32x4*)(ws + WS_CS + ((size_t)(8 * lw + i) * 112 + 7 * j) * 128 + cl * 16);
  SCHED_FENCE();
  auto wgu = [&](int s) {
#pragma unroll
    for (int i = 0; i < 7; ++i) blds16(rws, lane * 16, WS_WGU + (j * 32 * 7 + s * 28 + lw * 7 + i) * 1024, smem + (s % 5) * P4_SLOT + (lw * 7 + i) * 1024);
  };
  const rsrc_t rwd = make_rsrc(wd, F * D * 2);
  const unsigned vo = wrow_voff(lane);
  auto wdn = [&](int it) {
#pragma unroll
    for (int i = 0; i < 8; ++i) {
      const int pc = lw * 8 + i, kh = pc >> 4, rr = (pc & 15) * 8;
      blds16(rwd, vo, (896 * kh + 128 * p5_rot(it, rot) + rr) * 1024 + c * 128, smem + p5_slot(it) + pc * 1024);
    }
  };
  wgu(0); wgu(1);
  BARRIER();
  BARRIER();
  wgu(2); wgu(3); wgu(4);
  {
    f32x4 a = cv[0];
#pragma unroll
    for (int i = 1; i < 8; ++i) a += cv[i];
    if (lane < 56) *(f32x4*)(smem + L_CS + lw * 896 + lane * 16) = a;
  }
  WAIT_LGKM0(); BARRIER();
  if (lw == 0 && lane < 56) {
    const char* t = smem + L_CS + lane * 16;
    *(f32x4*)(smem + L_CS + lane * 16) = *(const f32x4*)t + *(const f32x4*)(t + 896) + *(const f32x4*)(t + 1792) + *(const f32x4*)(t + 2688);
  }
  WAIT_VM(28); WAIT_LGKM0(); BARRIER();
  WAIT_VM(21); BARRIER(); wgu(5);
  WAIT_VM(21); BARRIER(); wgu(6);
  WAIT_VM(21); BARRIER(); wgu(7);
  WAIT_VM(21); BARRIER();
  WAIT_VM(14); BARRIER(); wdn(0);
  WAIT_VM(15); BARRIER();
  WAIT_VM(8); BARRIER(); wdn(1);
  BARRIER(); wdn(2); wdn(3);
  BARRIER();
  WAIT_VM(24); BARRIER();
  WAIT_VM(16); BARRIER(); wdn(4);
  WAIT_VM(16); BARRIER(); wdn(5);
  WAIT_VM(16); BARRIER(); wdn(6);
  WAIT_VM(16); BARRIER();
  WAIT_VM(8); BARRIER();
  WAIT_VM(0); BARRIER();
  BARRIER();
}

extern "C" __global__ __launch_bounds__(512, 2) void k_main(char* __restrict__ ws, const char* __restrict__ x, const char* __restrict__ wo,
                                                 const char* __restrict__ wd, char* __restrict__ out) {
  extern __shared__ __attribute__((aligned(16))) char smem[];
  const int w = __builtin_amdgcn_readfirstlane(threadIdx.x >> 6), lane = threadIdx.x & 63;
  const rsrc_t rws = make_rsrc(ws, WS_BYTES);
  phase_attn(smem, w, lane, ws, rws, wo);
  const int xcd = blockIdx.x & 7, i = blockIdx.x >> 3;
  const int tp = 4 * xcd + (i & 3), c = i >> 2;
  const int p4 = 8 * (xcd & 1) + (i & 7), j4 = 4 * (xcd >> 1) + (i >> 3);
  const int rot = (c + 2 * (i & 3)) % 7;
  if (w >= 4) loader_345(smem, w - 4, lane, ws, rws, wd, j4, c, rot);
  else {
    oproj_compute(smem, w, lane, ws, rws, x, tp, c);
    gateup_compute(smem, w, lane, ws, rws, p4, j4);
    down_compute(smem, w, lane, ws, rws, out, tp, c, rot);
  }
}

extern "C" void kernel_launch(void* const* d_in, const int* in_sizes, int n_in, void* d_out, int out_size, void* d_ws, size_t ws_size,
                              hipStream_t stream) {
  const char* x = (const char*)d_in[0];
  const char* wqkv = (const char*)d_in[1];
  const char* wo = (const char*)d_in[2];
  const char* wg = (const char*)d_in[4];
  const char* wu = (const char*)d_in[5];
  const char* wd = (const char*)d_in[6];
  char* ws = (char*)d_ws;
  (void)hipFuncSetAttribute((const void*)k_qkv, hipFuncAttributeMaxDynamicSharedMemorySize, P1_LDS);
  (void)hipFuncSetAttribute((const void*)k_main, hipFuncAttributeMaxDynamicSharedMemorySize, MAIN_LDS);
  k_qkv<<<256, 512, P1_LDS, stream>>>(x, wqkv, wg, wu, ws);
  k_main<<<256, 512, MAIN_LDS, stream>>>(ws, x, wo, wd, (char*)d_out);
}
```

```cpp
#include <hip/hip_runtime.h>
#include <hip/hip_bf16.h>
#include <stdint.h>

typedef __attribute__((ext_vector_type(8))) __bf16 bf16x8;
typedef __attribute__((ext_vector_type(2))) __bf16 bf16x2;
typedef __attribute__((ext_vector_type(16))) float f32x16;
typedef __attribute__((ext_vector_type(8))) float f32x8;
typedef __attribute__((ext_vector_type(4))) float f32x4;
typedef __attribute__((ext_vector_type(2))) float f32x2;
typedef __attribute__((ext_vector_type(4))) unsigned u32x4;
typedef __attribute__((ext_vector_type(2))) unsigned u32x2;
typedef __attribute__((ext_vector_type(4))) short s16x4;
typedef __attribute__((address_space(3))) s16x4* lds_s16x4;
typedef __attribute__((address_space(3))) void* lds_vp;
typedef const __attribute__((address_space(1))) void* g_vp;

#define DI __device__ __forceinline__
#define MFMA32(a, b, c) __builtin_amdgcn_mfma_f32_32x32x16_bf16((a), (b), (c), 0, 0, 0)
#define BARRIER() asm volatile("s_barrier" ::: "memory")
#define WAIT_VM(n) asm volatile("s_waitcnt vmcnt(" #n ")" ::: "memory")
#define WAIT_LGKM0() asm volatile("s_waitcnt lgkmcnt(0)" ::: "memory")
#define SCHED_FENCE() __builtin_amdgcn_sched_barrier(0)

constexpr int S = 2048, D = 512, F = 1792;
constexpr float LN_EPS = 1e-5f;
constexpr float Q_SCALE = 0.25f * 1.4426950408889634f;

constexpr unsigned WS_Q = 0;
constexpr unsigned WS_K = WS_Q + (2u << 20);
constexpr unsigned WS_V = WS_K + (2u << 20);
constexpr unsigned WS_O = WS_V + (2u << 20);
constexpr unsigned WS_H = WS_O + (2u << 20);
constexpr unsigned WS_ST = WS_H + (2u << 20);
constexpr unsigned WS_M = WS_ST + (256u << 10);
constexpr unsigned WS_WGU = WS_M + (7u << 20);
constexpr unsigned WS_CS = WS_WGU + (3584u << 10);
constexpr unsigned WS_CNT = WS_CS + (448u << 10);
constexpr unsigned WS_BYTES = WS_CNT + 48 * 128;

typedef __amdgpu_buffer_rsrc_t rsrc_t;
typedef __attribute__((address_space(3))) const bf16x8* lds_fp;
typedef __attribute__((address_space(1))) unsigned gu32;
constexpr int SC1 = 16;

DI rsrc_t make_rsrc(const void* p, unsigned bytes) { return __builtin_amdgcn_make_buffer_rsrc((void*)p, 0, bytes, 0x00020000); }
DI void glds16(const void* g, void* l) { __builtin_amdgcn_global_load_lds((g_vp)g, (lds_vp)l, 16, 0, 0); }
DI void blds16(rsrc_t rs, unsigned voff, unsigned soff, char* l) { __builtin_amdgcn_raw_ptr_buffer_load_lds(rs, (lds_vp)l, 16, voff, soff, 0, 0); }
DI u32x4 ld16_dev(rsrc_t rs, unsigned voff, unsigned soff) { return __builtin_amdgcn_raw_buffer_load_b128(rs, voff, soff, SC1); }
DI void st16_dev(rsrc_t rs, unsigned voff, unsigned soff, u32x4 v) { __builtin_amdgcn_raw_buffer_store_b128(v, rs, voff + soff, 0, SC1); }
#define P1_AUXBITS 16
DI void st16_p1(rsrc_t rs, unsigned voff, unsigned soff, u32x4 v) { __builtin_amdgcn_raw_buffer_store_b128(v, rs, voff + soff, 0, P1_AUXBITS); }
DI bf16x8 lds_frag(const char* p) { return *(const bf16x8*)p; }
DI bf16x8 lds_frag_a(unsigned a) { return *(lds_fp)(size_t)a; }
DI unsigned lds_addr(const char* p) { return (unsigned)(size_t)(const __attribute__((address_space(3))) char*)p; }
DI unsigned opaque(unsigned v) { asm volatile("" : "+v"(v)); return v; }
DI bf16x8 tr_frag(const char* p0, const char* p1) {
  s16x4 lo = __builtin_amdgcn_ds_read_tr16_b64_v4i16((lds_s16x4)p0);
  s16x4 hi = __builtin_amdgcn_ds_read_tr16_b64_v4i16((lds_s16x4)p1);
  return __builtin_bit_cast(bf16x8, __builtin_shufflevector(lo, hi, 0, 1, 2, 3, 4, 5, 6, 7));
}
DI bf16x8 tr_frag_a(unsigned a0, unsigned a1) {
  s16x4 lo = __builtin_amdgcn_ds_read_tr16_b64_v4i16((lds_s16x4)(size_t)a0);
  s16x4 hi = __builtin_amdgcn_ds_read_tr16_b64_v4i16((lds_s16x4)(size_t)a1);
  return __builtin_bit_cast(bf16x8, __builtin_shufflevector(lo, hi, 0, 1, 2, 3, 4, 5, 6, 7));
}
DI bf16x8 pack8(float a0, float a1, float a2, float a3, float a4, float a5, float a6, float a7) {
  f32x8 v = {a0, a1, a2, a3, a4, a5, a6, a7};
  return __builtin_convertvector(v, bf16x8);
}
DI u32x4 as_u4(bf16x8 v) { return __builtin_bit_cast(u32x4, v); }
DI bf16x8 as_frag(u32x4 v) { return __builtin_bit_cast(bf16x8, v); }
DI float bf_lo(unsigned u) { return __uint_as_float(u << 16); }
DI float bf_hi(unsigned u) { return __uint_as_float(u & 0xffff0000u); }
DI float half_swap_sum(float v) {
  auto r = __builtin_amdgcn_permlane32_swap(__float_as_uint(v), __float_as_uint(v), false, false);
  return __uint_as_float(r[0]) + __uint_as_float(r[1]);
}
DI float half_swap_max(float v) {
  auto r = __builtin_amdgcn_permlane32_swap(__float_as_uint(v), __float_as_uint(v), false, false);
  return fmaxf(__uint_as_float(r[0]), __uint_as_float(r[1]));
}
DI void add_f32(float& a, float v) { asm("v_add_f32 %0, %0, %1" : "+v"(a) : "v"(v)); }
DI void fma_sq(float& a, float v) { asm("v_fma_f32 %0, %1, %1, %0" : "+v"(a) : "v"(v)); }
DI float colsum_frag(bf16x8 f, float acc) {
  const bf16x2 ones = {(__bf16)1.0f, (__bf16)1.0f};
  const u32x4 u = as_u4(f);
#pragma unroll
  for (int i = 0; i < 4; ++i) { const unsigned e = u[i]; acc = __builtin_amdgcn_fdot2_f32_bf16(__builtin_bit_cast(bf16x2, e), ones, acc, false); }
  return acc;
}

DI void signal_panel(char* ws, int phase, int panel) {
  __hip_atomic_fetch_add((gu32*)(ws + WS_CNT + (phase * 16 + panel) * 128), 1u, __ATOMIC_RELAXED, __HIP_MEMORY_SCOPE_AGENT);
}
DI void wait_panel(char* ws, int phase, int panel) {
  gu32* word = (gu32*)(ws + WS_CNT + (phase * 16 + panel) * 128);
  for (unsigned spins = 0; spins < (1u << 19); ++spins) {
    if ((unsigned)__builtin_amdgcn_readfirstlane(__hip_atomic_load(word, __ATOMIC_RELAXED, __HIP_MEMORY_SCOPE_AGENT)) >= 16u) break;
  }
  asm volatile("" ::: "memory");
}
constexpr int P1_SLOT = 28672, P1_NSLOT = 5, P1_AUX = P1_SLOT * P1_NSLOT, P1_PREP = P1_AUX + 4096;
constexpr int P1_LDS = P1_PREP + 16384;

template <int VMASK>
DI void p1_compute(char* smem, int w, int lane, int p, int j, char* ws) {
  const int r = lane & 31, h = lane >> 5;
  const int row = 32 * w + r, sw = (r >> 1) & 7;
  const unsigned lb = lds_addr(smem);
  unsigned xa[4][3], wa[3];
#pragma unroll
  for (int ks = 0; ks < 4; ++ks) {
    const unsigned o = lb + row * 128 + (((2 * ks + h) ^ sw) << 4);
#pragma unroll
    for (int b = 0; b < 3; ++b) xa[ks][b] = opaque(o + b * 2 * P1_SLOT);
  }
  {
    const int q = (lane & 15) >> 2, pp = lane & 3, blk = (lane >> 4) & 1;
    const unsigned o = lb + 16384 + (8 * h + q) * 192 + (2 * blk + (pp >> 1)) * 16 + 8 * (pp & 1);
#pragma unroll
    for (int b = 0; b < 3; ++b) wa[b] = opaque(o + b * 2 * P1_SLOT);
  }
  f32x16 acc[3] = {};
  float cs[3] = {0.f, 0.f, 0.f};
  bf16x8 xf[3], wf[3][3];
  auto rdx = [&](int g) { const int sl = (g >> 2) % P1_NSLOT; xf[g % 3] = lds_frag_a(xa[g & 3][sl >> 1] + (sl & 1) * P1_SLOT); };
  auto rdw = [&](int g, int c) {
    const int sl = (g >> 2) % P1_NSLOT;
    const unsigned a = wa[sl >> 1] + (sl & 1) * P1_SLOT + (16 * (g & 3)) * 192 + c * 64;
    wf[g % 3][c] = tr_frag_a(a, a + 4 * 192);
  };
  auto mm = [&](int g, int c) {
    if ((VMASK >> c) & 1) acc[c] = MFMA32(xf[g % 3], wf[g % 3][c], acc[c]);
    else acc[c] = MFMA32(wf[g % 3][c], xf[g % 3], acc[c]);
  };
  BARRIER();
#pragma unroll
  for (int g = 0; g < 2; ++g) { rdx(g); rdw(g, 0); rdw(g, 1); rdw(g, 2); }
  SCHED_FENCE();
#pragma unroll
  for (int g = 0; g < 32; ++g) {
    const int n = g + 2;
    mm(g, 0); SCHED_FENCE();
    if (n < 32) {
      if ((n & 3) == 0) { WAIT_LGKM0(); BARRIER(); }
      rdx(n); rdw(n, 0); SCHED_FENCE();
    }
    mm(g, 1); SCHED_FENCE();
    if (n < 32) { rdw(n, 1); rdw(n, 2); SCHED_FENCE(); }
    mm(g, 2); SCHED_FENCE();
    if ((g & 3) == w) {
#pragma unroll
      for (int c = 0; c < 3; ++c) cs[c] = colsum_frag(wf[g % 3][c], cs[c]);
    }
    SCHED_FENCE();
  }
  float* csp = (float*)(smem + P1_AUX);
  float* cst = csp + 384;
  f32x2* st = (f32x2*)(smem + P1_AUX + 2048);
#pragma unroll
  for (int c = 0; c < 3; ++c) { float t = half_swap_sum(cs[c]); if (h == 0) csp[w * 96 + c * 32 + r] = t; }
  WAIT_LGKM0(); BARRIER();
  if (w < 2) { int t = w * 64 + lane; if (t < 96) cst[t] = csp[t] + csp[96 + t] + csp[192 + t] + csp[288 + t]; }
  WAIT_LGKM0(); BARRIER();
  const int tb = 4 * p + w;
  const rsrc_t rws = make_rsrc(ws, WS_BYTES);
  const f32x2 mine = st[row];
#pragma unroll
  for (int c = 0; c < 3; ++c) {
    const int c0 = 96 * j + 32 * c;
    if ((VMASK >> c) & 1) {
      const float csv = cst[32 * c + r];
      float v[16];
#pragma unroll
      for (int i = 0; i < 16; ++i) { f32x2 t = st[32 * w + (i & 3) + 8 * (i >> 2) + 4 * h]; v[i] = acc[c][i] * t[0] - t[1] * csv; }
      const unsigned dst = WS_V + (((c0 - 1024) >> 5) * 128 + 2 * tb) * 1024;
      st16_p1(rws, lane * 16, dst, as_u4(pack8(v[0], v[1], v[2], v[3], v[4], v[5], v[6], v[7])));
      st16_p1(rws, lane * 16, dst + 1024, as_u4(pack8(v[8], v[9], v[10], v[11], v[12], v[13], v[14], v[15])));
    } else {
      const bool isq = c0 < 512;
      const float sc = isq ? Q_SCALE : 1.0f;
      const float a = mine[0] * sc, b = mine[1] * sc;
      float v[16];
#pragma unroll
      for (int g = 0; g < 4; ++g) {
        f32x4 cv = *(const f32x4*)(cst + 32 * c + 8 * g + 4 * h);
#pragma unroll
        for (int i = 0; i < 4; ++i) v[4 * g + i] = acc[c][4 * g + i] * a - b * cv[i];
      }
      const int head = (c0 & 511) >> 4;
      const unsigned dst = (isq ? WS_Q : WS_K) + (head * 64 + tb) * 1024;
      st16_p1(rws, lane * 16, dst, as_u4(pack8(v[0], v[1], v[2], v[3], v[4], v[5], v[6], v[7])));
      st16_p1(rws, lane * 16, dst + 65536, as_u4(pack8(v[8], v[9], v[10], v[11], v[12], v[13], v[14], v[15])));
    }
  }
}

DI void p1_helper(char* smem, int lw, int lane, int p, int j, const char* x, const char* wqkv, const char* wg, const char* wu, char* ws) {
  const rsrc_t rx = make_rsrc(x, S * D * 2), rw = make_rsrc(wqkv, D * 3 * D * 2);
  unsigned xo[4], wo_[3];
#pragma unroll
  for (int i = 0; i < 4; ++i) {
    int row = 32 * lw + 8 * i + (lane >> 3);
    xo[i] = (128 * p + row) * 1024 + (((lane & 7) ^ ((row >> 1) & 7)) << 4);
  }
#pragma unroll
  for (int i = 0; i < 3; ++i) { int g = 64 * (3 * lw + i) + lane; int row = g / 12, c = g % 12; wo_[i] = row * 3072 + 192 * j + c * 16; }
  auto issue = [&](int s) {
    char* sl = smem + (s % P1_NSLOT) * P1_SLOT;
#pragma unroll
    for (int i = 0; i < 4; ++i) blds16(rx, xo[i], s * 128, sl + (4 * lw + i) * 1024);
#pragma unroll
    for (int i = 0; i < 3; ++i) blds16(rw, wo_[i], s * 64 * 3072, sl + 16384 + (3 * lw + i) * 1024);
  };
  issue(0); issue(1); issue(2);
  const int b = blockIdx.x, cg = b % 28, pks = 4 * (b / 28) + lw;
  const bool prep = b < 224;
  char* stg = smem + P1_PREP + lw * 4096;
  auto prep_issue = [&]() {
    const rsrc_t rg = make_rsrc(wg, D * F * 2), ru = make_rsrc(wu, D * F * 2);
    const unsigned off = (16 * pks + (lane >> 3)) * 3584 + cg * 128 + (lane & 7) * 16;
    blds16(rg, off, 0, stg); blds16(rg, off, 8 * 3584, stg + 1024);
    blds16(ru, off, 0, stg + 2048); blds16(ru, off, 8 * 3584, stg + 3072);
  };
  const int r = lane & 31, h = lane >> 5, row = 32 * lw + r, sw = (r >> 1) & 7;
  float sum0 = 0.f, sum1 = 0.f, sq0 = 0.f, sq1 = 0.f;
  auto piece = [&](int s, int i) {
    char* sl = smem + (s % P1_NSLOT) * P1_SLOT;
    if (i < 4) blds16(rx, xo[i], s * 128, sl + (4 * lw + i) * 1024);
    else blds16(rw, wo_[i - 4], s * 64 * 3072, sl + 16384 + (3 * lw + i - 4) * 1024);
  };
  auto step = [&](int s, int n) {
    const char* sl = smem + (s % P1_NSLOT) * P1_SLOT + row * 128;
    u32x4 u[4];
#pragma unroll
    for (int c = 0; c < 4; ++c) u[c] = *(const u32x4*)(sl + (((4 * h + c) ^ sw) << 4));
    SCHED_FENCE();
#pragma unroll
    for (int c = 0; c < 4; ++c) {
      if (n < 8) { piece(n, 2 * c); if (c < 3) piece(n, 2 * c + 1); }
      SCHED_FENCE();
#pragma unroll
      for (int i = 0; i < 4; ++i) {
        const float lo = bf_lo(u[c][i]), hi = bf_hi(u[c][i]);
        add_f32(sum0, lo); add_f32(sum1, hi); fma_sq(sq0, lo); fma_sq(sq1, hi);
      }
      SCHED_FENCE();
    }
  };
  WAIT_VM(14); BARRIER();
  step(0, 3); WAIT_VM(14); WAIT_LGKM0(); BARRIER();
  step(1, 4); WAIT_VM(14); WAIT_LGKM0(); BARRIER();
  step(2, 5); WAIT_VM(14); WAIT_LGKM0(); BARRIER();
  step(3, 6); WAIT_VM(14); WAIT_LGKM0(); BARRIER();
  step(4, 7); WAIT_VM(14); WAIT_LGKM0(); BARRIER();
  if (prep) { prep_issue(); step(5, 8); WAIT_VM(11); } else { step(5, 8); WAIT_VM(7); }
  WAIT_LGKM0(); BARRIER();
  step(6, 8); if (prep) WAIT_VM(4); else WAIT_VM(0);
  WAIT_LGKM0(); BARRIER();
  step(7, 8);
  const float s1 = half_swap_sum(sum0 + sum1), s2 = half_swap_sum(sq0 + sq1);
  const float mean = s1 * (1.0f / D);
  const float var = fmaxf(s2 * (1.0f / D) - mean * mean, 0.f);
  const float rstd = 1.0f / sqrtf(var + LN_EPS);
  f32x2* st = (f32x2*)(smem + P1_AUX + 2048);
  if (h == 0) st[row] = f32x2{rstd, mean * rstd};
  WAIT_LGKM0(); BARRIER();
  BARRIER();
  if (prep) {
    WAIT_VM(0);
    const int blk = (lane >> 4) & 1, q = (lane & 15) >> 2, pp = lane & 3;
    const char* bs = stg + blk * 2048 + (4 * h + q) * 128 + (pp >> 1) * 16 + 8 * (pp & 1);
#pragma unroll
    for (int i = 0; i < 4; ++i) {
      const int fs = 4 * cg + i, jj = fs / 7, t = fs % 7;
      const bf16x8 f = tr_frag(bs + i * 32, bs + 8 * 128 + i * 32);
      st16_p1(make_rsrc(ws, WS_BYTES), lane * 16, WS_WGU + ((jj * 32 + pks) * 7 + t) * 1024, as_u4(f));
      const float c = half_swap_sum(colsum_frag(f, 0.f));
      if (h == 0) *(float*)(ws + WS_CS + ((size_t)pks * 112 + fs) * 128 + r * 4) = c;
    }
  }
}

__global__ __launch_bounds__(512, 2) void k_qkv(const char* __restrict__ x, const char* __restrict__ wqkv, const char* __restrict__ wg,
                                                const char* __restrict__ wu, char* __restrict__ ws) {
  extern __shared__ __attribute__((aligned(16))) char smem[];
  const int w = __builtin_amdgcn_readfirstlane(threadIdx.x >> 6), lane = threadIdx.x & 63;
  const int xcd = blockIdx.x & 7, i = blockIdx.x >> 3;
  const int p = 4 * (xcd & 3) + (i & 3), j = 8 * (xcd >> 2) + (i >> 2);
  if (blockIdx.x == 255 && threadIdx.x < 48) *(unsigned*)(ws + WS_CNT + threadIdx.x * 128) = 0u;
  if (w >= 4) p1_helper(smem, w - 4, lane, p, j, x, wqkv, wg, wu, ws);
  else if (j <= 9) p1_compute<0>(smem, w, lane, p, j, ws);
  else if (j == 10) p1_compute<4>(smem, w, lane, p, j, ws);
  else p1_compute<7>(smem, w, lane, p, j, ws);
}

constexpr int P2_K = 0, P2_V = 24576, P2_VP = 1040, P2_Q = P2_V + 24 * P2_VP;
constexpr int L_WO = 77824;
constexpr int L_RED = 143360;
constexpr int L_CS = L_RED + 16384;
constexpr int MAIN_LDS = 163840;

template <bool FULL>
DI void attn_task(const char* smem, int lane, int hh, int wq, int qb, rsrc_t rws, int head) {
  const int r = lane & 31, h = lane >> 5;
  const char* kb_ = smem + P2_K + (hh * 12 + wq) * 1024 + lane * 16;
  const bool valid = (r >> 4) == hh;
  const char* vb_ = smem + P2_V + 2 * wq * P2_VP + (valid ? lane * 16 : 1024);
  const bf16x8 qf = lds_frag(smem + P2_Q + (hh * 4 + wq) * 1024 + lane * 16);
  const f32x16 zero = {};
  const int t0 = FULL ? 1 : ((8 - qb) > 1 ? (8 - qb) : 1);
  const bool have_first = FULL || qb >= 8;
  f32x16 sm;
  {
    const f32x16 sd = MFMA32(lds_frag(kb_ + 8 * 1024), qf, zero);
    f32x16 sf;
    if (have_first) sf = MFMA32(lds_frag(kb_), qf, zero);
    else { for (int i = 0; i < 16; ++i) sf[i] = -1e30f; }
#pragma unroll
    for (int i = 0; i < 16; ++i) { const int jj = (i & 3) + 8 * (i >> 2) + 4 * h; sm[i] = jj <= r ? sd[i] : sf[i]; }
  }
  float m = sm[0];
#pragma unroll
  for (int i = 1; i < 16; ++i) m = fmaxf(m, sm[i]);
  if (FULL) {
#pragma unroll
    for (int t = 1; t < 8; ++t) {
      const f32x16 s = MFMA32(lds_frag(kb_ + t * 1024), qf, zero);
#pragma unroll
      for (int i = 0; i < 16; i += 2) m = fmaxf(fmaxf(m, s[i]), s[i + 1]);
    }
  } else {
    for (int t = t0; t < 8; ++t) {
      const f32x16 s = MFMA32(lds_frag(kb_ + t * 1024), qf, zero);
#pragma unroll
      for (int i = 0; i < 16; i += 2) m = fmaxf(fmaxf(m, s[i]), s[i + 1]);
    }
  }
  m = half_swap_max(m);
  f32x16 negm;
#pragma unroll
  for (int i = 0; i < 16; ++i) negm[i] = -m;
  f32x16 o = zero;
  {
    float pv[16];
#pragma unroll
    for (int i = 0; i < 16; ++i) pv[i] = __builtin_amdgcn_exp2f(sm[i] - m);
    u32x4 pa = __builtin_bit_cast(u32x4, pack8(pv[0], pv[1], pv[2], pv[3], pv[4], pv[5], pv[6], pv[7]));
    u32x4 pb = __builtin_bit_cast(u32x4, pack8(pv[8], pv[9], pv[10], pv[11], pv[12], pv[13], pv[14], pv[15]));
    u32x4 da, db;
#pragma unroll
    for (int i = 0; i < 4; ++i) {
      const int ja = ((2 * i) & 3) + 8 * ((2 * i) >> 2) + 4 * h;
      const unsigned ma = (ja <= r ? 0xffffu : 0u) | (ja + 1 <= r ? 0xffff0000u : 0u);
      const unsigned mb = (ja + 16 <= r ? 0xffffu : 0u) | (ja + 17 <= r ? 0xffff0000u : 0u);
      da[i] = pa[i] & ma; db[i] = pb[i] & mb;
    }
    o = MFMA32(lds_frag(vb_ + 16 * P2_VP), __builtin_bit_cast(bf16x8, da), o);
    o = MFMA32(lds_frag(vb_ + 17 * P2_VP), __builtin_bit_cast(bf16x8, db), o);
    if (have_first) {
      o = MFMA32(lds_frag(vb_), __builtin_bit_cast(bf16x8, pa ^ da), o);
      o = MFMA32(lds_frag(vb_ + P2_VP), __builtin_bit_cast(bf16x8, pb ^ db), o);
    }
  }
  auto tile = [&](int t) {
    const f32x16 s = MFMA32(lds_frag(kb_ + t * 1024), qf, negm);
    float pv[16];
#pragma unroll
    for (int i = 0; i < 16; ++i) pv[i] = __builtin_amdgcn_exp2f(s[i]);
    o = MFMA32(lds_frag(vb_ + (2 * t) * P2_VP), pack8(pv[0], pv[1], pv[2], pv[3], pv[4], pv[5], pv[6], pv[7]), o);
    o = MFMA32(lds_frag(vb_ + (2 * t + 1) * P2_VP), pack8(pv[8], pv[9], pv[10], pv[11], pv[12], pv[13], pv[14], pv[15]), o);
  };
  if (FULL) {
#pragma unroll
    for (int t = 1; t < 8; ++t) tile(t);
  } else {
    for (int t = t0; t < 8; ++t) tile(t);
  }
  const float l = hh ? o[0] : o[8];
  const float inv = 1.0f / l;
  float ov[8];
#pragma unroll
  for (int i = 0; i < 8; ++i) ov[i] = (hh ? o[8 + i] : o[i]) * inv;
  st16_dev(rws, lane * 16, WS_O + (((qb >> 1) * 32 + head) * 2 + (qb & 1)) * 1024, as_u4(pack8(ov[0], ov[1], ov[2], ov[3], ov[4], ov[5], ov[6], ov[7])));
}

DI unsigned wrow_voff(int lane) { return (lane >> 3) * 1024 + (((lane & 7) ^ (((lane >> 4) & 1) << 2)) << 4); }
DI unsigned wtr_off(int lane, int nt) {
  const int h = lane >> 5, blk = (lane >> 4) & 1, q = (lane & 15) >> 2, pp = lane & 3, sw = (q >> 1) & 1;
  return (4 * h + q) * 128 + (2 * blk + (pp >> 1) + 4 * (nt ^ sw)) * 16 + 8 * (pp & 1);
}

DI void phase_attn(char* smem, int w, int lane, char* ws, rsrc_t rws, const char* wo) {
  const int xcd = blockIdx.x & 7, i = blockIdx.x >> 3;
  const int hp = 2 * xcd + (i & 1), p = i >> 1;
  const int kb0 = 4 * p - 8;
  const int hh = w >> 2, wq = w & 3, qb = 4 * p + wq, head = 2 * hp + hh;
  blds16(rws, lane * 16, WS_Q + (head * 64 + qb) * 1024, smem + P2_Q + w * 1024);
#pragma unroll
  for (int n = 0; n < 3; ++n) {
    const int t = wq * 3 + n, sv = w * 3 + n;
    const int kt = kb0 + t < 0 ? 0 : kb0 + t, vt = 2 * kb0 + sv < 0 ? 0 : 2 * kb0 + sv;
    blds16(rws, lane * 16, WS_K + (head * 64 + kt) * 1024, smem + P2_K + (hh * 12 + t) * 1024);
    blds16(rws, lane * 16, WS_V + (hp * 128 + vt) * 1024, smem + P2_V + sv * P2_VP);
  }
  if (threadIdx.x < 24) *(u32x4*)(smem + P2_V + threadIdx.x * P2_VP + 1024) = u32x4{0x3f803f80u, 0x3f803f80u, 0x3f803f80u, 0x3f803f80u};
  WAIT_VM(0); WAIT_LGKM0(); BARRIER();
  {
    const rsrc_t rwo = make_rsrc(wo, D * D * 2);
    const int c = i >> 2;
    const unsigned vo = wrow_voff(lane);
#pragma unroll
    for (int n = 0; n < 8; ++n) blds16(rwo, vo, (8 * w + n) * 8192 + c * 128, smem + L_WO + (8 * w + n) * 1024);
  }
  if (qb >= 8) attn_task<true>(smem, lane, hh, wq, qb, rws, head);
  else attn_task<false>(smem, lane, hh, wq, qb, rws, head);
  WAIT_VM(0); WAIT_LGKM0(); BARRIER();
  if (threadIdx.x == 0) signal_panel(ws, 0, p);
}

template <int NACC>
DI f32x16 pair_reduce(char* smem, int w, int lane, const f32x16 (&acc)[NACC]) {
  const int kh = w >> 1;
  const f32x16 send = kh ? acc[0] : acc[1];
  f32x16 a = kh ? acc[1] : acc[0];
#pragma unroll
  for (int g = 0; g < 4; ++g)
    *(f32x4*)(smem + L_RED + (w * 4 + g) * 1024 + lane * 16) = f32x4{send[4 * g], send[4 * g + 1], send[4 * g + 2], send[4 * g + 3]};
  WAIT_LGKM0(); BARRIER();
#pragma unroll
  for (int g = 0; g < 4; ++g) {
    const f32x4 v = *(const f32x4*)(smem + L_RED + ((w ^ 2) * 4 + g) * 1024 + lane * 16);
    a[4 * g] += v[0]; a[4 * g + 1] += v[1]; a[4 * g + 2] += v[2]; a[4 * g + 3] += v[3];
  }
  return a;
}

DI void oproj_compute(char* smem, int w, int lane, char* ws, rsrc_t rws, const char* x, int tp, int c) {
  const int r = lane & 31, h = lane >> 5, tt = w & 1, kh = w >> 1;
  const int tb = 2 * tp + tt, ntile = 2 * c + kh;
  u32x2 rx[4];
#pragma unroll
  for (int g = 0; g < 4; ++g) rx[g] = *(const u32x2*)(x + (size_t)(32 * tb + r) * 1024 + (32 * ntile + 8 * g + 4 * h) * 2);
  if (w == 0) wait_panel(ws, 0, tp >> 1);
  BARRIER();
  u32x4 xf[16];
#pragma unroll
  for (int g = 0; g < 16; ++g) xf[g] = ld16_dev(rws, lane * 16, WS_O + ((tp * 32 + 16 * kh + g) * 2 + tt) * 1024);
  const unsigned lb = lds_addr(smem) + L_WO + kh * 32768;
  const unsigned a0 = opaque(lb + wtr_off(lane, 0)), a1 = opaque(lb + wtr_off(lane, 1));
  bf16x8 wf[3][2];
  auto rd = [&](int g) {
    wf[g % 3][0] = tr_frag_a(a0 + g * 2048, a0 + g * 2048 + 1024);
    wf[g % 3][1] = tr_frag_a(a1 + g * 2048, a1 + g * 2048 + 1024);
  };
  f32x16 acc[2] = {};
  rd(0); rd(1);
  SCHED_FENCE();
#pragma unroll
  for (int g = 0; g < 16; ++g) {
    acc[0] = MFMA32(wf[g % 3][0], as_frag(xf[g]), acc[0]); SCHED_FENCE();
    if (g + 2 < 16) { rd(g + 2); SCHED_FENCE(); }
    acc[1] = MFMA32(wf[g % 3][1], as_frag(xf[g]), acc[1]); SCHED_FENCE();
  }
  f32x16 a = pair_reduce<2>(smem, w, lane, acc);
#pragma unroll
  for (int g = 0; g < 4; ++g) {
    a[4 * g] += bf_lo(rx[g][0]); a[4 * g + 1] += bf_hi(rx[g][0]); a[4 * g + 2] += bf_lo(rx[g][1]); a[4 * g + 3] += bf_hi(rx[g][1]);
  }
  float s1 = 0.f, s2 = 0.f;
#pragma unroll
  for (int s = 0; s < 2; ++s) {
    const u32x4 u = as_u4(pack8(a[8 * s], a[8 * s + 1], a[8 * s + 2], a[8 * s + 3], a[8 * s + 4], a[8 * s + 5], a[8 * s + 6], a[8 * s + 7]));
    st16_dev(rws, lane * 16, WS_H + (tb * 32 + 2 * ntile + s) * 1024, u);
#pragma unroll
    for (int i = 0; i < 4; ++i) { const float lo = bf_lo(u[i]), hi = bf_hi(u[i]); s1 += lo + hi; s2 += lo * lo + hi * hi; }
  }
  s1 = half_swap_sum(s1); s2 = half_swap_sum(s2);
  if (h == 0) __builtin_amdgcn_raw_buffer_store_b64(u32x2{__float_as_uint(s1), __float_as_uint(s2)}, rws, r * 8, WS_ST + (tb * 16 + ntile) * 256, SC1);
  WAIT_VM(0); BARRIER();
  if (threadIdx.x == 0) signal_panel(ws, 1, tp >> 1);
}

#define XD4 12
constexpr int P4_SLOT = 28672, P4_XD = XD4;

DI void gateup_compute(char* smem, int w, int lane, char* ws, rsrc_t rws, int p, int j) {
  const int r = lane & 31, h = lane >> 5, tb = 4 * p + w;
  if (w == 0) wait_panel(ws, 1, p);
  BARRIER();
  u32x4 xf[32];
  auto ldx = [&](int g) { xf[g] = ld16_dev(rws, lane * 16, WS_H + (tb * 32 + g) * 1024); };
  ldx(0); ldx(1);
  u32x2 sv[8];
#pragma unroll
  for (int i = 0; i < 8; ++i) sv[i] = __builtin_amdgcn_raw_buffer_load_b64(rws, (8 * h + i) * 256 + r * 8, WS_ST + tb * 4096, SC1);
#pragma unroll
  for (int g = 2; g < P4_XD; ++g) ldx(g);
  const unsigned lb = lds_addr(smem) + lane * 16;
  unsigned ba[3];
#pragma unroll
  for (int b = 0; b < 3; ++b) ba[b] = opaque(lb + b * 2 * P4_SLOT);
  bf16x8 wf[2][7];
  auto rdw = [&](int g, int t) {
    const int sl = (g >> 2) % 5;
    wf[g & 1][t] = lds_frag_a(ba[sl >> 1] + (sl & 1) * P4_SLOT + ((g & 3) * 7 + t) * 1024);
  };
  f32x16 acc[7] = {};
#pragma unroll
  for (int t = 0; t < 7; ++t) rdw(0, t);
  float s1 = 0.f, s2 = 0.f;
  SCHED_FENCE();
#pragma unroll
  for (int g = 0; g < 32; ++g) {
    const int n = g + 1;
#pragma unroll
    for (int t = 0; t < 7; ++t) {
      acc[t] = MFMA32(wf[g & 1][t], as_frag(xf[g]), acc[t]); SCHED_FENCE();
      if (t == 0 && n < 32 && (n & 3) == 0) { WAIT_LGKM0(); BARRIER(); }
      if (t == 0 && n == 32) { WAIT_LGKM0(); BARRIER(); }
      if (n < 32) rdw(n, t);
      if (t == 1 && g + P4_XD < 32) ldx(g + P4_XD);
      if (g == 2 && t == 2) {
#pragma unroll
        for (int i = 0; i < 8; ++i) { const unsigned e0 = sv[i][0], e1 = sv[i][1]; s1 += __uint_as_float(e0); s2 += __uint_as_float(e1); }
      }
      SCHED_FENCE();
    }
  }
  s1 = half_swap_sum(s1); s2 = half_swap_sum(s2);
  const float mean = s1 * (1.0f / D);
  const float rstd = 1.0f / sqrtf(fmaxf(s2 * (1.0f / D) - mean * mean, 0.f) + LN_EPS);
  const float mr = mean * rstd;
  const float* cst = (const float*)(smem + L_CS);
  const unsigned dst = WS_M + (((tb >> 1) * 112 + 7 * j) * 2 + (tb & 1)) * 1024;
#pragma unroll
  for (int t = 0; t < 7; ++t) {
    float v[8];
#pragma unroll
    for (int g = 0; g < 2; ++g) {
      const f32x4 cg = *(const f32x4*)(cst + 32 * t + 8 * g + 4 * h), cu = *(const f32x4*)(cst + 32 * t + 16 + 8 * g + 4 * h);
#pragma unroll
      for (int i = 0; i < 4; ++i) {
        const float gv = acc[t][4 * g + i] * rstd - mr * cg[i], uv = acc[t][8 + 4 * g + i] * rstd - mr * cu[i];
        v[4 * g + i] = fmaxf(gv, 0.f) * uv;
      }
    }
    st16_dev(rws, lane * 16, dst + t * 2048, as_u4(pack8(v[0], v[1], v[2], v[3], v[4], v[5], v[6], v[7])));
  }
  WAIT_VM(0); BARRIER();
  if (threadIdx.x == 0) signal_panel(ws, 2, p);
}

#define XD5 10
constexpr int P5_SLOT = 32768, P5_XD = XD5;
DI constexpr int p5_slot(int it) { return ((it + 3) & 3) * P5_SLOT; }

DI int p5_rot(int it, int rot) { const int v = it + rot; return v >= 7 ? v - 7 : v; }

DI void down_compute(char* smem, int w, int lane, char* ws, rsrc_t rws, char* out, int tp, int c, int rot) {
  const int r = lane & 31, h = lane >> 5, tt = w & 1, kh = w >> 1;
  const int tb = 2 * tp + tt, ntile = 2 * c + kh;
  u32x4 rh[2];
#pragma unroll
  for (int s = 0; s < 2; ++s) rh[s] = ld16_dev(rws, lane * 16, WS_H + (tb * 32 + 2 * ntile + s) * 1024);
  if (w == 0) wait_panel(ws, 2, tp >> 1);
  BARRIER();
  u32x4 xf[56];
  unsigned mb[7];
#pragma unroll
  for (int it = 0; it < 7; ++it) mb[it] = WS_M + ((tp * 112 + 56 * kh + 8 * p5_rot(it, rot)) * 2 + tt) * 1024;
  auto ldx = [&](int g) { xf[g] = ld16_dev(rws, lane * 16, mb[g >> 3] + (g & 7) * 2048); };
#pragma unroll
  for (int g = 0; g < P5_XD; ++g) ldx(g);
  const unsigned lb = lds_addr(smem) + kh * 16384;
  unsigned a0[2], a1[2];
#pragma unroll
  for (int b = 0; b < 2; ++b) { a0[b] = opaque(lb + wtr_off(lane, 0) + b * 65536); a1[b] = opaque(lb + wtr_off(lane, 1) + b * 65536); }
  bf16x8 wf[3][2];
  auto rd = [&](int g) {
    const int o = p5_slot(g >> 3) + (g & 7) * 2048, b = o >> 16, oo = o & 65535;
    wf[g % 3][0] = tr_frag_a(a0[b] + oo, a0[b] + oo + 1024);
    wf[g % 3][1] = tr_frag_a(a1[b] + oo, a1[b] + oo + 1024);
  };
  f32x16 acc[2] = {};
  rd(0); rd(1);
  SCHED_FENCE();
#pragma unroll
  for (int g = 0; g < 56; ++g) {
    const int n = g + 2;
    acc[0] = MFMA32(wf[g % 3][0], as_frag(xf[g]), acc[0]); SCHED_FENCE();
    if (n < 56) {
      if ((n & 7) == 0) { WAIT_LGKM0(); BARRIER(); }
      rd(n);
    }
    if (g + P5_XD < 56) ldx(g + P5_XD);
    SCHED_FENCE();
    acc[1] = MFMA32(wf[g % 3][1], as_frag(xf[g]), acc[1]); SCHED_FENCE();
  }
  f32x16 a = pair_reduce<2>(smem, w, lane, acc);
  unsigned pk[8];
#pragma unroll
  for (int s = 0; s < 2; ++s) {
#pragma unroll
    for (int i = 0; i < 4; ++i) { a[8 * s + 2 * i] += bf_lo(rh[s][i]); a[8 * s + 2 * i + 1] += bf_hi(rh[s][i]); }
    const u32x4 u = as_u4(pack8(a[8 * s], a[8 * s + 1], a[8 * s + 2], a[8 * s + 3], a[8 * s + 4], a[8 * s + 5], a[8 * s + 6], a[8 * s + 7]));
#pragma unroll
    for (int i = 0; i < 4; ++i) pk[4 * s + i] = u[i];
  }
  char* dst = out + (size_t)(32 * tb + r) * 1024 + (32 * ntile + 8 * h) * 2;
#pragma unroll
  for (int s = 0; s < 2; ++s) {
    auto q0 = __builtin_amdgcn_permlane32_swap(pk[4 * s], pk[4 * s + 2], false, false);
    auto q1 = __builtin_amdgcn_permlane32_swap(pk[4 * s + 1], pk[4 * s + 3], false, false);
    *(u32x4*)(dst + s * 32) = u32x4{q0[0], q1[0], q0[1], q1[1]};
  }
}

DI void loader_345(char* smem, int lw, int lane, char* ws, rsrc_t rws, const char* wd, int j, int c, int rot) {
  {
    f32x4 a = {0.f, 0.f, 0.f, 0.f};
    if (lane < 56) {
      f32x4 v[8];
#pragma unroll
      for (int i = 0; i < 8; ++i) v[i] = *(const f32x4*)(ws + WS_CS + ((size_t)(8 * lw + i) * 112 + 7 * j) * 128 + lane * 16);
#pragma unroll
      for (int i = 0; i < 8; ++i) a += v[i];
      *(f32x4*)(smem + L_CS + lw * 896 + lane * 16) = a;
    }
    WAIT_VM(0);
  }
  auto wgu = [&](int s) {
#pragma unroll
    for (int i = 0; i < 7; ++i) blds16(rws, lane * 16, WS_WGU + (j * 32 * 7 + s * 28 + lw * 7 + i) * 1024, smem + (s % 5) * P4_SLOT + (lw * 7 + i) * 1024);
  };
  const rsrc_t rwd = make_rsrc(wd, F * D * 2);
  const unsigned vo = wrow_voff(lane);
  auto wdn = [&](int it) {
#pragma unroll
    for (int i = 0; i < 8; ++i) {
      const int pc = lw * 8 + i, kh = pc >> 4, rr = (pc & 15) * 8;
      blds16(rwd, vo, (896 * kh + 128 * p5_rot(it, rot) + rr) * 1024 + c * 128, smem + p5_slot(it) + pc * 1024);
    }
  };
  wgu(0); wgu(1);
  WAIT_LGKM0(); BARRIER();
  BARRIER();
  wgu(2); wgu(3); wgu(4);
  if (lw == 0 && lane < 56) {
    const char* t = smem + L_CS + lane * 16;
    *(f32x4*)(smem + L_CS + lane * 16) = *(const f32x4*)t + *(const f32x4*)(t + 896) + *(const f32x4*)(t + 1792) + *(const f32x4*)(t + 2688);
  }
  WAIT_LGKM0(); BARRIER();
  WAIT_VM(28); BARRIER();
  WAIT_VM(21); BARRIER(); wgu(5);
  WAIT_VM(21); BARRIER(); wgu(6);
  WAIT_VM(21); BARRIER(); wgu(7);
  WAIT_VM(21); BARRIER();
  WAIT_VM(14); BARRIER(); wdn(0);
  WAIT_VM(15); BARRIER();
  WAIT_VM(8); BARRIER(); wdn(1);
  BARRIER(); wdn(2); wdn(3);
  BARRIER();
  WAIT_VM(24); BARRIER();
  WAIT_VM(16); BARRIER(); wdn(4);
  WAIT_VM(16); BARRIER(); wdn(5);
  WAIT_VM(16); BARRIER(); wdn(6);
  WAIT_VM(16); BARRIER();
  WAIT_VM(8); BARRIER();
  WAIT_VM(0); BARRIER();
  BARRIER();
}

__global__ __launch_bounds__(512, 2) void k_main(char* __restrict__ ws, const char* __restrict__ x, const char* __restrict__ wo,
                                                 const char* __restrict__ wd, char* __restrict__ out) {
  extern __shared__ __attribute__((aligned(16))) char smem[];
  const int w = __builtin_amdgcn_readfirstlane(threadIdx.x >> 6), lane = threadIdx.x & 63;
  const rsrc_t rws = make_rsrc(ws, WS_BYTES);
  phase_attn(smem, w, lane, ws, rws, wo);
  const int xcd = blockIdx.x & 7, i = blockIdx.x >> 3;
  const int tp = 4 * xcd + (i & 3), c = i >> 2;
  const int p4 = 8 * (xcd & 1) + (i & 7), j4 = 4 * (xcd >> 1) + (i >> 3);
  const int rot = (c + 2 * (i & 3)) % 7;
  if (w >= 4) loader_345(smem, w - 4, lane, ws, rws, wd, j4, c, rot);
  else {
    oproj_compute(smem, w, lane, ws, rws, x, tp, c);
    gateup_compute(smem, w, lane, ws, rws, p4, j4);
    down_compute(smem, w, lane, ws, rws, out, tp, c, rot);
  }
}

extern "C" void kernel_launch(void* const* d_in, const int* in_sizes, int n_in, void* d_out, int out_size, void* d_ws, size_t ws_size,
                              hipStream_t stream) {
  const char* x = (const char*)d_in[0];
  const char* wqkv = (const char*)d_in[1];
  const char* wo = (const char*)d_in[2];
  const char* wg = (const char*)d_in[4];
  const char* wu = (const char*)d_in[5];
  const char* wd = (const char*)d_in[6];
  char* ws = (char*)d_ws;
  (void)hipFuncSetAttribute((const void*)k_qkv, hipFuncAttributeMaxDynamicSharedMemorySize, P1_LDS);
  (void)hipFuncSetAttribute((const void*)k_main, hipFuncAttributeMaxDynamicSharedMemorySize, MAIN_LDS);
  k_qkv<<<256, 512, P1_LDS, stream>>>(x, wqkv, wg, wu, ws);
  k_main<<<256, 512, MAIN_LDS, stream>>>(ws, x, wo, wd, (char*)d_out);
}
```

```cpp
#include <hip/hip_runtime.h>
#include <hip/hip_bf16.h>
#include <stdint.h>

typedef __attribute__((ext_vector_type(8))) __bf16 bf16x8;
typedef __attribute__((ext_vector_type(2))) __bf16 bf16x2;
typedef __attribute__((ext_vector_type(16))) float f32x16;
typedef __attribute__((ext_vector_type(8))) float f32x8;
typedef __attribute__((ext_vector_type(4))) float f32x4;
typedef __attribute__((ext_vector_type(2))) float f32x2;
typedef __attribute__((ext_vector_type(4))) unsigned u32x4;
typedef __attribute__((ext_vector_type(2))) unsigned u32x2;
typedef __attribute__((ext_vector_type(4))) short s16x4;
typedef __attribute__((address_space(3))) s16x4* lds_s16x4;
typedef __attribute__((address_space(3))) void* lds_vp;
typedef const __attribute__((address_space(1))) void* g_vp;

#define POLL_GAP "4"
#define POLL_SLEEP ""
#define DI __device__ __forceinline__
#define MFMA32(a, b, c) __builtin_amdgcn_mfma_f32_32x32x16_bf16((a), (b), (c), 0, 0, 0)
#define BARRIER() asm volatile("s_barrier" ::: "memory")
#define WAIT_VM(n) asm volatile("s_waitcnt vmcnt(" #n ")" ::: "memory")
#define WAIT_LGKM0() asm volatile("s_waitcnt lgkmcnt(0)" ::: "memory")
#define SCHED_FENCE() __builtin_amdgcn_sched_barrier(0)

constexpr int S = 2048, D = 512, F = 1792;
constexpr float LN_EPS = 1e-5f;
constexpr float Q_SCALE = 0.25f * 1.4426950408889634f;

constexpr unsigned WS_Q = 0;
constexpr unsigned WS_K = WS_Q + (2u << 20);
constexpr unsigned WS_V = WS_K + (2u << 20);
constexpr unsigned WS_O = WS_V + (2u << 20);
constexpr unsigned WS_H = WS_O + (2u << 20);
constexpr unsigned WS_ST = WS_H + (2u << 20);
constexpr unsigned WS_M = WS_ST + (256u << 10);
constexpr unsigned WS_WGU = WS_M + (7u << 20);
constexpr unsigned WS_CS = WS_WGU + (3584u << 10);
constexpr unsigned WS_CNT = WS_CS + (448u << 10);
constexpr unsigned WS_BYTES = WS_CNT + 768 * 128;

typedef __amdgpu_buffer_rsrc_t rsrc_t;
typedef __attribute__((address_space(3))) const bf16x8* lds_fp;
typedef __attribute__((address_space(1))) unsigned gu32;
constexpr int SC1 = 16;
constexpr unsigned QKV_CODE_BYTES = 35840, MAIN_CODE_BYTES = 26112;

DI rsrc_t make_rsrc(const void* p, unsigned bytes) { return __builtin_amdgcn_make_buffer_rsrc((void*)p, 0, bytes, 0x00020000); }
DI void glds16(const void* g, void* l) { __builtin_amdgcn_global_load_lds((g_vp)g, (lds_vp)l, 16, 0, 0); }
DI void blds16(rsrc_t rs, unsigned voff, unsigned soff, char* l) { __builtin_amdgcn_raw_ptr_buffer_load_lds(rs, (lds_vp)l, 16, voff, soff, 0, 0); }
DI u32x4 ld16_dev(rsrc_t rs, unsigned voff, unsigned soff) { return __builtin_amdgcn_raw_buffer_load_b128(rs, voff, soff, SC1); }
DI void st16_dev(rsrc_t rs, unsigned voff, unsigned soff, u32x4 v) { __builtin_amdgcn_raw_buffer_store_b128(v, rs, voff + soff, 0, SC1); }
#define P1_AUXBITS 16
DI void st16_p1(rsrc_t rs, unsigned voff, unsigned soff, u32x4 v) { __builtin_amdgcn_raw_buffer_store_b128(v, rs, voff + soff, 0, P1_AUXBITS); }
DI bf16x8 lds_frag(const char* p) { return *(const bf16x8*)p; }
DI bf16x8 lds_frag_a(unsigned a) { return *(lds_fp)(size_t)a; }
DI unsigned lds_addr(const char* p) { return (unsigned)(size_t)(const __attribute__((address_space(3))) char*)p; }
DI unsigned touch(const char* g) { unsigned v; asm volatile("global_load_dword %0, %1, off" : "=v"(v) : "v"(g)); return v; }
#define KERNEL_ADDR(dst, sym) do { unsigned lo_, hi_; \
  asm volatile("s_getpc_b64 s[40:41]\n\ts_add_u32 s40, s40, " #sym "@rel32@lo+4\n\ts_addc_u32 s41, s41, " #sym "@rel32@hi+12\n\ts_mov_b32 %0, s40\n\ts_mov_b32 %1, s41" \
               : "=s"(lo_), "=s"(hi_) :: "s40", "s41", "scc"); \
  dst = ((unsigned long long)hi_ << 32) | lo_; } while (0)
DI void dma_piece(u32x4 rs, unsigned voff, unsigned soff, unsigned lds) {
  asm volatile("s_mov_b32 m0, %0\n\ts_nop 0\n\tbuffer_load_dwordx4 %1, %2, %3 offen lds" :: "s"(lds), "v"(voff), "s"(rs), "s"(soff));
}
DI unsigned opaque(unsigned v) { asm volatile("" : "+v"(v)); return v; }
DI bf16x8 tr_frag(const char* p0, const char* p1) {
  s16x4 lo = __builtin_amdgcn_ds_read_tr16_b64_v4i16((lds_s16x4)p0);
  s16x4 hi = __builtin_amdgcn_ds_read_tr16_b64_v4i16((lds_s16x4)p1);
  return __builtin_bit_cast(bf16x8, __builtin_shufflevector(lo, hi, 0, 1, 2, 3, 4, 5, 6, 7));
}
DI bf16x8 tr_frag_a(unsigned a0, unsigned a1) {
  s16x4 lo = __builtin_amdgcn_ds_read_tr16_b64_v4i16((lds_s16x4)(size_t)a0);
  s16x4 hi = __builtin_amdgcn_ds_read_tr16_b64_v4i16((lds_s16x4)(size_t)a1);
  return __builtin_bit_cast(bf16x8, __builtin_shufflevector(lo, hi, 0, 1, 2, 3, 4, 5, 6, 7));
}
DI bf16x8 pack8(float a0, float a1, float a2, float a3, float a4, float a5, float a6, float a7) {
  f32x8 v = {a0, a1, a2, a3, a4, a5, a6, a7};
  return __builtin_convertvector(v, bf16x8);
}
DI u32x4 as_u4(bf16x8 v) { return __builtin_bit_cast(u32x4, v); }
DI bf16x8 as_frag(u32x4 v) { return __builtin_bit_cast(bf16x8, v); }
DI float bf_lo(unsigned u) { return __uint_as_float(u << 16); }
DI float bf_hi(unsigned u) { return __uint_as_float(u & 0xffff0000u); }
DI float half_swap_sum(float v) {
  auto r = __builtin_amdgcn_permlane32_swap(__float_as_uint(v), __float_as_uint(v), false, false);
  return __uint_as_float(r[0]) + __uint_as_float(r[1]);
}
DI float half_swap_max(float v) {
  auto r = __builtin_amdgcn_permlane32_swap(__float_as_uint(v), __float_as_uint(v), false, false);
  return fmaxf(__uint_as_float(r[0]), __uint_as_float(r[1]));
}
DI void add_f32(float& a, float v) { asm("v_add_f32 %0, %0, %1" : "+v"(a) : "v"(v)); }
DI void fma_sq(float& a, float v) { asm("v_fma_f32 %0, %1, %1, %0" : "+v"(a) : "v"(v)); }
DI float colsum_frag(bf16x8 f, float acc) {
  const bf16x2 ones = {(__bf16)1.0f, (__bf16)1.0f};
  const u32x4 u = as_u4(f);
#pragma unroll
  for (int i = 0; i < 4; ++i) { const unsigned e = u[i]; acc = __builtin_amdgcn_fdot2_f32_bf16(__builtin_bit_cast(bf16x2, e), ones, acc, false); }
  return acc;
}

DI void signal_panel(char* ws, int phase, int panel) {
  if (threadIdx.x < 16)
    __hip_atomic_fetch_add((gu32*)(ws + WS_CNT + ((phase * 16 + panel) * 16 + threadIdx.x) * 128), 1u, __ATOMIC_RELAXED, __HIP_MEMORY_SCOPE_AGENT);
}
struct Polls { unsigned v[4]; };
DI void wait_panel(char* ws, int phase, int panel, int replica, int w, Polls& p) {
  const char* word = ws + WS_CNT + ((phase * 16 + panel) * 16 + replica) * 128;
  unsigned left = 1u << 17, seen;
  unsigned zero = 0;
  asm volatile(
      "s_cmp_lg_u32 %8, 0\n\ts_cbranch_scc1 2f\n"
      "1:\n\tglobal_load_dword %0, %6, %7 sc1\n\ts_waitcnt vmcnt(0)\n\tv_readfirstlane_b32 %5, %0\n\ts_cmp_ge_u32 %5, 16\n\ts_cbranch_scc1 2f\n\t" POLL_SLEEP
      "s_sub_u32 %4, %4, 1\n\ts_cmp_lg_u32 %4, 0\n\ts_cbranch_scc1 1b\n"
      "2:"
      : "=&v"(p.v[0]), "=&v"(p.v[1]), "=&v"(p.v[2]), "=&v"(p.v[3]), "+s"(left), "=&s"(seen)
      : "v"(zero), "s"(word), "s"(w)
      : "scc", "memory");
  return;
#define POLL_STEP(k) "s_waitcnt vmcnt(3)\n\tv_readfirstlane_b32 %5, %" #k "\n\ts_cmp_ge_u32 %5, 16\n\ts_cbranch_scc1 2f\n\tglobal_load_dword %" #k ", %6, %7 sc1\n\t"
  asm volatile(
      "s_cmp_lg_u32 %8, 0\n\ts_cbranch_scc1 2f\n\t"
      "global_load_dword %0, %6, %7 sc1\n\ts_sleep " POLL_GAP "\n\t"
      "global_load_dword %1, %6, %7 sc1\n\ts_sleep " POLL_GAP "\n\t"
      "global_load_dword %2, %6, %7 sc1\n\ts_sleep " POLL_GAP "\n\t"
      "global_load_dword %3, %6, %7 sc1\n"
      "1:\n\t" POLL_STEP(0) POLL_STEP(1) POLL_STEP(2) POLL_STEP(3)
      "s_sub_u32 %4, %4, 1\n\ts_cmp_lg_u32 %4, 0\n\ts_cbranch_scc1 1b\n"
      "2:"
      : "=&v"(p.v[0]), "=&v"(p.v[1]), "=&v"(p.v[2]), "=&v"(p.v[3]), "+s"(left), "=&s"(seen)
      : "v"(zero), "s"(word), "s"(w)
      : "scc", "memory");
#undef POLL_STEP
}
DI void retire(const Polls& p) { asm volatile("; retire %0 %1 %2 %3" :: "v"(p.v[0]), "v"(p.v[1]), "v"(p.v[2]), "v"(p.v[3])); }
constexpr int P1_SLOT = 28672, P1_NSLOT = 5, P1_AUX = P1_SLOT * P1_NSLOT, P1_PREP = P1_AUX + 4096;
constexpr int P1_LDS = P1_PREP + 16384;

template <int VMASK>
DI void p1_compute(char* smem, int w, int lane, int p, int j, const char* wqkv, char* ws, const char* wo, const char* wd, const char* out) {
  const int r = lane & 31, h = lane >> 5;
  const int row = 32 * w + r, sw = (r >> 1) & 7;
  const unsigned lb = lds_addr(smem);
  unsigned xa[4][3], wa[3];
#pragma unroll
  for (int ks = 0; ks < 4; ++ks) {
    const unsigned o = lb + row * 128 + (((2 * ks + h) ^ sw) << 4);
#pragma unroll
    for (int b = 0; b < 3; ++b) xa[ks][b] = opaque(o + b * 2 * P1_SLOT);
  }
  {
    const int q = (lane & 15) >> 2, pp = lane & 3, blk = (lane >> 4) & 1;
    const unsigned o = lb + 16384 + (8 * h + q) * 192 + (2 * blk + (pp >> 1)) * 16 + 8 * (pp & 1);
#pragma unroll
    for (int b = 0; b < 3; ++b) wa[b] = opaque(o + b * 2 * P1_SLOT);
  }
  f32x16 acc[3] = {};
  float cs[3] = {0.f, 0.f, 0.f};
  bf16x8 xf[3], wf[3][3];
  auto rdx = [&](int g) { const int sl = (g >> 2) % P1_NSLOT; xf[g % 3] = lds_frag_a(xa[g & 3][sl >> 1] + (sl & 1) * P1_SLOT); };
  auto rdw = [&](int g, int c) {
    const int sl = (g >> 2) % P1_NSLOT;
    const unsigned a = wa[sl >> 1] + (sl & 1) * P1_SLOT + (16 * (g & 3)) * 192 + c * 64;
    wf[g % 3][c] = tr_frag_a(a, a + 4 * 192);
  };
  auto mm = [&](int g, int c) {
    if ((VMASK >> c) & 1) acc[c] = MFMA32(xf[g % 3], wf[g % 3][c], acc[c]);
    else acc[c] = MFMA32(wf[g % 3][c], xf[g % 3], acc[c]);
  };
  const unsigned long long wa64 = (unsigned long long)wqkv;
  const u32x4 rw = {(unsigned)wa64, (unsigned)(wa64 >> 32) & 0xffffu, (unsigned)(D * 3 * D * 2), 0x00020000u};
  unsigned wo_[3];
#pragma unroll
  for (int i = 0; i < 3; ++i) { const int g = 64 * (3 * w + i) + lane; wo_[i] = (g / 12) * 3072 + 192 * j + (g % 12) * 16; }
  auto wpiece = [&](int s, int i) { dma_piece(rw, wo_[i], s * 64 * 3072, lb + (s % P1_NSLOT) * P1_SLOT + 16384 + (3 * w + i) * 1024); };
#pragma unroll
  for (int s = 0; s < 3; ++s) { wpiece(s, 0); wpiece(s, 1); wpiece(s, 2); }
  unsigned warm;
  {
    unsigned long long ka, kb;
    KERNEL_ADDR(ka, k_qkv); KERNEL_ADDR(kb, k_main);
    const unsigned i = blockIdx.x >> 3, v = 4 * w + (lane & 3);
    const char* src = v < 9 ? (const char*)ka + min((i * 9 + v) * 128u, QKV_CODE_BYTES - 128u) : (const char*)kb + min((i * 7 + v - 9) * 128u, MAIN_CODE_BYTES - 128u);
    asm volatile("global_load_dword %0, %1, off" : "=v"(warm) : "v"(src));
  }
  unsigned warm2 = 0;
  WAIT_VM(7); BARRIER();
#pragma unroll
  for (int g = 0; g < 2; ++g) { rdx(g); rdw(g, 0); rdw(g, 1); rdw(g, 2); }
  SCHED_FENCE();
#pragma unroll
  for (int g = 0; g < 32; ++g) {
    const int n = g + 2;
    mm(g, 0); SCHED_FENCE();
    if (n < 32) {
      if ((n & 3) == 0) {
        if (n <= 8) WAIT_VM(7); else if (n <= 20) WAIT_VM(6); else if (n == 24) WAIT_VM(3); else WAIT_VM(0);
        WAIT_LGKM0(); BARRIER();
      }
      rdx(n); rdw(n, 0); SCHED_FENCE();
    }
    mm(g, 1); SCHED_FENCE();
    {
      const int m = g < 2 ? g : g + 2;
      const int s = g < 2 ? 3 : (m >> 2) + 3, i = g < 2 ? g : (m & 3);
      if (s < 8 && i < 3) { wpiece(s, i); SCHED_FENCE(); }
      if (g == 1) { wpiece(3, 2); SCHED_FENCE(); }
    }
    if (n < 32) { rdw(n, 1); rdw(n, 2); SCHED_FENCE(); }
    mm(g, 2); SCHED_FENCE();
    if ((g & 3) == w) {
#pragma unroll
      for (int c = 0; c < 3; ++c) cs[c] = colsum_frag(wf[g % 3][c], cs[c]);
    }
    SCHED_FENCE();
  }
  asm volatile("; warm %0" :: "v"(warm));
  float* csp = (float*)(smem + P1_AUX);
  float* cst = csp + 384;
  f32x2* st = (f32x2*)(smem + P1_AUX + 2048);
#pragma unroll
  for (int c = 0; c < 3; ++c) { float t = half_swap_sum(cs[c]); if (h == 0) csp[w * 96 + c * 32 + r] = t; }
  WAIT_LGKM0(); BARRIER();
  if (w < 2) { int t = w * 64 + lane; if (t < 96) cst[t] = csp[t] + csp[96 + t] + csp[192 + t] + csp[288 + t]; }
  WAIT_LGKM0(); BARRIER();
  const int tb = 4 * p + w;
  const rsrc_t rws = make_rsrc(ws, WS_BYTES);
  const f32x2 mine = st[row];
#pragma unroll
  for (int c = 0; c < 3; ++c) {
    const int c0 = 96 * j + 32 * c;
    if ((VMASK >> c) & 1) {
      const float csv = cst[32 * c + r];
      float v[16];
#pragma unroll
      for (int i = 0; i < 16; ++i) { f32x2 t = st[32 * w + (i & 3) + 8 * (i >> 2) + 4 * h]; v[i] = acc[c][i] * t[0] - t[1] * csv; }
      const unsigned dst = WS_V + (((c0 - 1024) >> 5) * 128 + 2 * tb) * 1024;
      st16_p1(rws, lane * 16, dst, as_u4(pack8(v[0], v[1], v[2], v[3], v[4], v[5], v[6], v[7])));
      st16_p1(rws, lane * 16, dst + 1024, as_u4(pack8(v[8], v[9], v[10], v[11], v[12], v[13], v[14], v[15])));
    } else {
      const bool isq = c0 < 512;
      const float sc = isq ? Q_SCALE : 1.0f;
      const float a = mine[0] * sc, b = mine[1] * sc;
      float v[16];
#pragma unroll
      for (int g = 0; g < 4; ++g) {
        f32x4 cv = *(const f32x4*)(cst + 32 * c + 8 * g + 4 * h);
#pragma unroll
        for (int i = 0; i < 4; ++i) v[4 * g + i] = acc[c][4 * g + i] * a - b * cv[i];
      }
      const int head = (c0 & 511) >> 4;
      const unsigned dst = (isq ? WS_Q : WS_K) + (head * 64 + tb) * 1024;
      st16_p1(rws, lane * 16, dst, as_u4(pack8(v[0], v[1], v[2], v[3], v[4], v[5], v[6], v[7])));
      st16_p1(rws, lane * 16, dst + 65536, as_u4(pack8(v[8], v[9], v[10], v[11], v[12], v[13], v[14], v[15])));
    }
  }
  asm volatile("; warm %0" :: "v"(warm2));
}

DI void p1_helper(char* smem, int lw, int lane, int p, int j, const char* x, const char* wg, const char* wu, char* ws) {
  const rsrc_t rx = make_rsrc(x, S * D * 2);
  unsigned xo[4];
#pragma unroll
  for (int i = 0; i < 4; ++i) {
    int row = 32 * lw + 8 * i + (lane >> 3);
    xo[i] = (128 * p + row) * 1024 + (((lane & 7) ^ ((row >> 1) & 7)) << 4);
  }
  auto issue = [&](int s) {
    char* sl = smem + (s % P1_NSLOT) * P1_SLOT;
#pragma unroll
    for (int i = 0; i < 4; ++i) blds16(rx, xo[i], s * 128, sl + (4 * lw + i) * 1024);
  };
  issue(0); issue(1); issue(2);
  const int b = blockIdx.x, cg = b % 28, pks = 4 * (b / 28) + lw;
  const bool prep = b < 224;
  char* stg = smem + P1_PREP + lw * 4096;
  auto prep_issue = [&]() {
    const rsrc_t rg = make_rsrc(wg, D * F * 2), ru = make_rsrc(wu, D * F * 2);
    const unsigned off = (16 * pks + (lane >> 3)) * 3584 + cg * 128 + (lane & 7) * 16;
    blds16(rg, off, 0, stg); blds16(rg, off, 8 * 3584, stg + 1024);
    blds16(ru, off, 0, stg + 2048); blds16(ru, off, 8 * 3584, stg + 3072);
  };
  const int r = lane & 31, h = lane >> 5, row = 32 * lw + r, sw = (r >> 1) & 7;
  bf16x8 pf_[4];
  auto prep_read = [&]() {
    const int blk = (lane >> 4) & 1, q = (lane & 15) >> 2, pp = lane & 3;
    const unsigned bs = lds_addr(stg) + blk * 2048 + (4 * h + q) * 128 + (pp >> 1) * 16 + 8 * (pp & 1);
    u32x2 t[8];
#define PREP_RD(i, off) asm volatile("ds_read_b64_tr_b16 %0, %1 offset:" #off : "=v"(t[i]) : "v"(bs))
    PREP_RD(0, 0); PREP_RD(1, 1024); PREP_RD(2, 32); PREP_RD(3, 1056); PREP_RD(4, 64); PREP_RD(5, 1088); PREP_RD(6, 96); PREP_RD(7, 1120);
#undef PREP_RD
    asm volatile("s_waitcnt lgkmcnt(0)" : "+v"(t[0]), "+v"(t[1]), "+v"(t[2]), "+v"(t[3]), "+v"(t[4]), "+v"(t[5]), "+v"(t[6]), "+v"(t[7]));
#pragma unroll
    for (int i = 0; i < 4; ++i) { const u32x2 lo = t[2 * i], hi = t[2 * i + 1]; pf_[i] = as_frag(u32x4{lo[0], lo[1], hi[0], hi[1]}); }
  };
  auto prep_frag = [&](int i) {
    const int fs = 4 * cg + i, jj = fs / 7, t = fs % 7;
    st16_p1(make_rsrc(ws, WS_BYTES), lane * 16, WS_WGU + ((jj * 32 + pks) * 7 + t) * 1024, as_u4(pf_[i]));
    const float c = half_swap_sum(colsum_frag(pf_[i], 0.f));
    if (h == 0) *(float*)(ws + WS_CS + ((size_t)pks * 112 + fs) * 128 + r * 4) = c;
  };
  float sum0 = 0.f, sum1 = 0.f, sq0 = 0.f, sq1 = 0.f;
  auto piece = [&](int s, int i) { blds16(rx, xo[i], s * 128, smem + (s % P1_NSLOT) * P1_SLOT + (4 * lw + i) * 1024); };
  auto step = [&](int s, int n) {
    const char* sl = smem + (s % P1_NSLOT) * P1_SLOT + row * 128;
    u32x4 u[4];
#pragma unroll
    for (int c = 0; c < 4; ++c) u[c] = *(const u32x4*)(sl + (((4 * h + c) ^ sw) << 4));
    SCHED_FENCE();
#pragma unroll
    for (int c = 0; c < 4; ++c) {
      if (n < 8) piece(n, c);
      SCHED_FENCE();
#pragma unroll
      for (int i = 0; i < 4; ++i) {
        const float lo = bf_lo(u[c][i]), hi = bf_hi(u[c][i]);
        add_f32(sum0, lo); add_f32(sum1, hi); fma_sq(sq0, lo); fma_sq(sq1, hi);
      }
      SCHED_FENCE();
    }
  };
  if (prep) {
    prep_issue();
    WAIT_VM(12); BARRIER();
    step(0, 3); WAIT_VM(12); WAIT_LGKM0(); BARRIER();
    step(1, 4); WAIT_VM(12); WAIT_LGKM0(); BARRIER();
  } else {
    WAIT_VM(8); BARRIER();
    step(0, 3); WAIT_VM(8); WAIT_LGKM0(); BARRIER();
    step(1, 4); WAIT_VM(8); WAIT_LGKM0(); BARRIER();
  }
  step(2, 5); WAIT_VM(8); WAIT_LGKM0(); BARRIER();
  step(3, 6); WAIT_VM(8); WAIT_LGKM0(); BARRIER();
  step(4, 7); WAIT_VM(8); WAIT_LGKM0(); BARRIER();
  step(5, 8); WAIT_VM(4);
  if (prep) prep_read();
  WAIT_LGKM0(); BARRIER();
  step(6, 8); WAIT_VM(0);
  if (prep) prep_frag(0);
  WAIT_LGKM0(); BARRIER();
  step(7, 8);
  const float s1 = half_swap_sum(sum0 + sum1), s2 = half_swap_sum(sq0 + sq1);
  const float mean = s1 * (1.0f / D);
  const float var = fmaxf(s2 * (1.0f / D) - mean * mean, 0.f);
  const float rstd = 1.0f / sqrtf(var + LN_EPS);
  f32x2* st = (f32x2*)(smem + P1_AUX + 2048);
  if (h == 0) st[row] = f32x2{rstd, mean * rstd};
  WAIT_LGKM0(); BARRIER();
  BARRIER();
  if (prep) prep_frag(1);
  if (prep) { prep_frag(2); prep_frag(3); }
}

extern "C" __global__ __launch_bounds__(512, 2) void k_qkv(const char* __restrict__ x, const char* __restrict__ wqkv, const char* __restrict__ wg,
                                                const char* __restrict__ wu, char* __restrict__ ws,
                                                const char* __restrict__ wo, const char* __restrict__ wd, const char* __restrict__ out) {
  extern __shared__ __attribute__((aligned(16))) char smem[];
  const int w = __builtin_amdgcn_readfirstlane(threadIdx.x >> 6), lane = threadIdx.x & 63;
  const int xcd = blockIdx.x & 7, i = blockIdx.x >> 3;
  const int p = 4 * (xcd & 3) + (i & 3), j = 8 * (xcd >> 2) + (i >> 2);
  if (threadIdx.x < 3) *(unsigned*)(ws + WS_CNT + (blockIdx.x * 3 + threadIdx.x) * 128) = 0u;
  if (w >= 4) p1_helper(smem, w - 4, lane, p, j, x, wg, wu, ws);
  else if (j <= 9) p1_compute<0>(smem, w, lane, p, j, wqkv, ws, wo, wd, out);
  else if (j == 10) p1_compute<4>(smem, w, lane, p, j, wqkv, ws, wo, wd, out);
  else p1_compute<7>(smem, w, lane, p, j, wqkv, ws, wo, wd, out);
}

constexpr int P2_K = 0, P2_V = 24576, P2_VP = 1040, P2_Q = P2_V + 24 * P2_VP;
constexpr int L_WO = 77824;
constexpr int L_RED = 143360;
constexpr int L_CS = L_RED + 16384;
constexpr int MAIN_LDS = 163840;

template <bool FULL>
DI void attn_task(const char* smem, int lane, int hh, int wq, int qb, rsrc_t rws, int head) {
  const int r = lane & 31, h = lane >> 5;
  const char* kb_ = smem + P2_K + (hh * 12 + wq) * 1024 + lane * 16;
  const bool valid = (r >> 4) == hh;
  const char* vb_ = smem + P2_V + 2 * wq * P2_VP + (valid ? lane * 16 : 1024);
  const bf16x8 qf = lds_frag(smem + P2_Q + (hh * 4 + wq) * 1024 + lane * 16);
  const f32x16 zero = {};
  const int t0 = FULL ? 1 : ((8 - qb) > 1 ? (8 - qb) : 1);
  const bool have_first = FULL || qb >= 8;
  f32x16 sm;
  {
    const f32x16 sd = MFMA32(lds_frag(kb_ + 8 * 1024), qf, zero);
    f32x16 sf;
    if (have_first) sf = MFMA32(lds_frag(kb_), qf, zero);
    else { for (int i = 0; i < 16; ++i) sf[i] = -1e30f; }
#pragma unroll
    for (int i = 0; i < 16; ++i) { const int jj = (i & 3) + 8 * (i >> 2) + 4 * h; sm[i] = jj <= r ? sd[i] : sf[i]; }
  }
  float m = sm[0];
#pragma unroll
  for (int i = 1; i < 16; ++i) m = fmaxf(m, sm[i]);
  if (FULL) {
#pragma unroll
    for (int t = 1; t < 8; ++t) {
      const f32x16 s = MFMA32(lds_frag(kb_ + t * 1024), qf, zero);
#pragma unroll
      for (int i = 0; i < 16; i += 2) m = fmaxf(fmaxf(m, s[i]), s[i + 1]);
    }
  } else {
    for (int t = t0; t < 8; ++t) {
      const f32x16 s = MFMA32(lds_frag(kb_ + t * 1024), qf, zero);
#pragma unroll
      for (int i = 0; i < 16; i += 2) m = fmaxf(fmaxf(m, s[i]), s[i + 1]);
    }
  }
  m = half_swap_max(m);
  f32x16 negm;
#pragma unroll
  for (int i = 0; i < 16; ++i) negm[i] = -m;
  f32x16 o = zero;
  {
    float pv[16];
#pragma unroll
    for (int i = 0; i < 16; ++i) pv[i] = __builtin_amdgcn_exp2f(sm[i] - m);
    u32x4 pa = __builtin_bit_cast(u32x4, pack8(pv[0], pv[1], pv[2], pv[3], pv[4], pv[5], pv[6], pv[7]));
    u32x4 pb = __builtin_bit_cast(u32x4, pack8(pv[8], pv[9], pv[10], pv[11], pv[12], pv[13], pv[14], pv[15]));
    u32x4 da, db;
#pragma unroll
    for (int i = 0; i < 4; ++i) {
      const int ja = ((2 * i) & 3) + 8 * ((2 * i) >> 2) + 4 * h;
      const unsigned ma = (ja <= r ? 0xffffu : 0u) | (ja + 1 <= r ? 0xffff0000u : 0u);
      const unsigned mb = (ja + 16 <= r ? 0xffffu : 0u) | (ja + 17 <= r ? 0xffff0000u : 0u);
      da[i] = pa[i] & ma; db[i] = pb[i] & mb;
    }
    o = MFMA32(lds_frag(vb_ + 16 * P2_VP), __builtin_bit_cast(bf16x8, da), o);
    o = MFMA32(lds_frag(vb_ + 17 * P2_VP), __builtin_bit_cast(bf16x8, db), o);
    if (have_first) {
      o = MFMA32(lds_frag(vb_), __builtin_bit_cast(bf16x8, pa ^ da), o);
      o = MFMA32(lds_frag(vb_ + P2_VP), __builtin_bit_cast(bf16x8, pb ^ db), o);
    }
  }
  auto tile = [&](int t) {
    const f32x16 s = MFMA32(lds_frag(kb_ + t * 1024), qf, negm);
    float pv[16];
#pragma unroll
    for (int i = 0; i < 16; ++i) pv[i] = __builtin_amdgcn_exp2f(s[i]);
    o = MFMA32(lds_frag(vb_ + (2 * t) * P2_VP), pack8(pv[0], pv[1], pv[2], pv[3], pv[4], pv[5], pv[6], pv[7]), o);
    o = MFMA32(lds_frag(vb_ + (2 * t + 1) * P2_VP), pack8(pv[8], pv[9], pv[10], pv[11], pv[12], pv[13], pv[14], pv[15]), o);
  };
  if (FULL) {
#pragma unroll
    for (int t = 1; t < 8; ++t) tile(t);
  } else {
    for (int t = t0; t < 8; ++t) tile(t);
  }
  const float l = hh ? o[0] : o[8];
  const float inv = 1.0f / l;
  float ov[8];
#pragma unroll
  for (int i = 0; i < 8; ++i) ov[i] = (hh ? o[8 + i] : o[i]) * inv;
  st16_dev(rws, lane * 16, WS_O + (((qb >> 1) * 32 + head) * 2 + (qb & 1)) * 1024, as_u4(pack8(ov[0], ov[1], ov[2], ov[3], ov[4], ov[5], ov[6], ov[7])));
}

DI unsigned wrow_voff(int lane) { return (lane >> 3) * 1024 + (((lane & 7) ^ (((lane >> 4) & 1) << 2)) << 4); }
DI unsigned wtr_off(int lane, int nt) {
  const int h = lane >> 5, blk = (lane >> 4) & 1, q = (lane & 15) >> 2, pp = lane & 3, sw = (q >> 1) & 1;
  return (4 * h + q) * 128 + (2 * blk + (pp >> 1) + 4 * (nt ^ sw)) * 16 + 8 * (pp & 1);
}

DI void phase_attn(char* smem, int w, int lane, char* ws, rsrc_t rws, const char* wo) {
  const int xcd = blockIdx.x & 7, i = blockIdx.x >> 3;
  const int hp = 2 * xcd + (i & 1), p = i >> 1;
  const int kb0 = 4 * p - 8;
  const int hh = w >> 2, wq = w & 3, qb = 4 * p + wq, head = 2 * hp + hh;
  blds16(rws, lane * 16, WS_Q + (head * 64 + qb) * 1024, smem + P2_Q + w * 1024);
#pragma unroll
  for (int n = 0; n < 3; ++n) {
    const int t = wq * 3 + n, sv = w * 3 + n;
    const int kt = kb0 + t < 0 ? 0 : kb0 + t, vt = 2 * kb0 + sv < 0 ? 0 : 2 * kb0 + sv;
    blds16(rws, lane * 16, WS_K + (head * 64 + kt) * 1024, smem + P2_K + (hh * 12 + t) * 1024);
    blds16(rws, lane * 16, WS_V + (hp * 128 + vt) * 1024, smem + P2_V + sv * P2_VP);
  }
  if (threadIdx.x < 24) *(u32x4*)(smem + P2_V + threadIdx.x * P2_VP + 1024) = u32x4{0x3f803f80u, 0x3f803f80u, 0x3f803f80u, 0x3f803f80u};
  WAIT_VM(0); WAIT_LGKM0(); BARRIER();
  {
    const rsrc_t rwo = make_rsrc(wo, D * D * 2);
    const int c = i >> 2;
    const unsigned vo = wrow_voff(lane);
#pragma unroll
    for (int n = 0; n < 8; ++n) blds16(rwo, vo, (8 * w + n) * 8192 + c * 128, smem + L_WO + (8 * w + n) * 1024);
  }
  if (qb >= 8) attn_task<true>(smem, lane, hh, wq, qb, rws, head);
  else attn_task<false>(smem, lane, hh, wq, qb, rws, head);
  WAIT_VM(0); WAIT_LGKM0(); BARRIER();
  signal_panel(ws, 0, p);
}

template <int NACC>
DI f32x16 pair_reduce(char* smem, int w, int lane, const f32x16 (&acc)[NACC]) {
  const int kh = w >> 1;
  const f32x16 send = kh ? acc[0] : acc[1];
  f32x16 a = kh ? acc[1] : acc[0];
#pragma unroll
  for (int g = 0; g < 4; ++g)
    *(f32x4*)(smem + L_RED + (w * 4 + g) * 1024 + lane * 16) = f32x4{send[4 * g], send[4 * g + 1], send[4 * g + 2], send[4 * g + 3]};
  WAIT_LGKM0(); BARRIER();
#pragma unroll
  for (int g = 0; g < 4; ++g) {
    const f32x4 v = *(const f32x4*)(smem + L_RED + ((w ^ 2) * 4 + g) * 1024 + lane * 16);
    a[4 * g] += v[0]; a[4 * g + 1] += v[1]; a[4 * g + 2] += v[2]; a[4 * g + 3] += v[3];
  }
  return a;
}

DI void oproj_compute(char* smem, int w, int lane, char* ws, rsrc_t rws, const char* x, int tp, int c) {
  const int r = lane & 31, h = lane >> 5, tt = w & 1, kh = w >> 1;
  const int tb = 2 * tp + tt, ntile = 2 * c + kh;
  u32x2 rx[4];
#pragma unroll
  for (int g = 0; g < 4; ++g) rx[g] = *(const u32x2*)(x + (size_t)(32 * tb + r) * 1024 + (32 * ntile + 8 * g + 4 * h) * 2);
  Polls pl;
  wait_panel(ws, 0, tp >> 1, (tp & 1) * 8 + c, w, pl);
  BARRIER();
  u32x4 xf[16];
#pragma unroll
  for (int g = 0; g < 16; ++g) xf[g] = ld16_dev(rws, lane * 16, WS_O + ((tp * 32 + 16 * kh + g) * 2 + tt) * 1024);
  const unsigned lb = lds_addr(smem) + L_WO + kh * 32768;
  const unsigned a0 = opaque(lb + wtr_off(lane, 0)), a1 = opaque(lb + wtr_off(lane, 1));
  bf16x8 wf[3][2];
  auto rd = [&](int g) {
    wf[g % 3][0] = tr_frag_a(a0 + g * 2048, a0 + g * 2048 + 1024);
    wf[g % 3][1] = tr_frag_a(a1 + g * 2048, a1 + g * 2048 + 1024);
  };
  f32x16 acc[2] = {};
  rd(0); rd(1);
  SCHED_FENCE();
#pragma unroll
  for (int g = 0; g < 16; ++g) {
    acc[0] = MFMA32(wf[g % 3][0], as_frag(xf[g]), acc[0]); SCHED_FENCE();
    if (g == 0) retire(pl);
    if (g + 2 < 16) { rd(g + 2); SCHED_FENCE(); }
    acc[1] = MFMA32(wf[g % 3][1], as_frag(xf[g]), acc[1]); SCHED_FENCE();
  }
  f32x16 a = pair_reduce<2>(smem, w, lane, acc);
#pragma unroll
  for (int g = 0; g < 4; ++g) {
    a[4 * g] += bf_lo(rx[g][0]); a[4 * g + 1] += bf_hi(rx[g][0]); a[4 * g + 2] += bf_lo(rx[g][1]); a[4 * g + 3] += bf_hi(rx[g][1]);
  }
  float s1 = 0.f, s2 = 0.f;
#pragma unroll
  for (int s = 0; s < 2; ++s) {
    const u32x4 u = as_u4(pack8(a[8 * s], a[8 * s + 1], a[8 * s + 2], a[8 * s + 3], a[8 * s + 4], a[8 * s + 5], a[8 * s + 6], a[8 * s + 7]));
    st16_dev(rws, lane * 16, WS_H + (tb * 32 + 2 * ntile + s) * 1024, u);
#pragma unroll
    for (int i = 0; i < 4; ++i) { const float lo = bf_lo(u[i]), hi = bf_hi(u[i]); s1 += lo + hi; s2 += lo * lo + hi * hi; }
  }
  s1 = half_swap_sum(s1); s2 = half_swap_sum(s2);
  if (h == 0) __builtin_amdgcn_raw_buffer_store_b64(u32x2{__float_as_uint(s1), __float_as_uint(s2)}, rws, r * 8, WS_ST + (tb * 16 + ntile) * 256, SC1);
  WAIT_VM(0); BARRIER();
  signal_panel(ws, 1, tp >> 1);
}

#define XD4 12
constexpr int P4_SLOT = 28672, P4_XD = XD4;

DI void gateup_compute(char* smem, int w, int lane, char* ws, rsrc_t rws, int p, int j) {
  const int r = lane & 31, h = lane >> 5, tb = 4 * p + w;
  Polls pl;
  wait_panel(ws, 1, p, j, w, pl);
  BARRIER();
  u32x4 xf[32];
  auto ldx = [&](int g) { xf[g] = ld16_dev(rws, lane * 16, WS_H + (tb * 32 + g) * 1024); };
  ldx(0); ldx(1);
  u32x2 sv[8];
#pragma unroll
  for (int i = 0; i < 8; ++i) sv[i] = __builtin_amdgcn_raw_buffer_load_b64(rws, (8 * h + i) * 256 + r * 8, WS_ST + tb * 4096, SC1);
#pragma unroll
  for (int g = 2; g < P4_XD; ++g) ldx(g);
  const unsigned lb = lds_addr(smem) + lane * 16;
  unsigned ba[3];
#pragma unroll
  for (int b = 0; b < 3; ++b) ba[b] = opaque(lb + b * 2 * P4_SLOT);
  bf16x8 wf[2][7];
  auto rdw = [&](int g, int t) {
    const int sl = (g >> 2) % 5;
    wf[g & 1][t] = lds_frag_a(ba[sl >> 1] + (sl & 1) * P4_SLOT + ((g & 3) * 7 + t) * 1024);
  };
  f32x16 acc[7] = {};
#pragma unroll
  for (int t = 0; t < 7; ++t) rdw(0, t);
  float s1 = 0.f, s2 = 0.f;
  SCHED_FENCE();
#pragma unroll
  for (int g = 0; g < 32; ++g) {
    const int n = g + 1;
#pragma unroll
    for (int t = 0; t < 7; ++t) {
      acc[t] = MFMA32(wf[g & 1][t], as_frag(xf[g]), acc[t]); SCHED_FENCE();
      if (g == 0 && t == 0) retire(pl);
      if (t == 0 && n < 32 && (n & 3) == 0) { WAIT_LGKM0(); BARRIER(); }
      if (t == 0 && n == 32) { WAIT_LGKM0(); BARRIER(); }
      if (n < 32) rdw(n, t);
      if (t == 1 && g + P4_XD < 32) ldx(g + P4_XD);
      if (g == 2 && t == 2) {
#pragma unroll
        for (int i = 0; i < 8; ++i) { const unsigned e0 = sv[i][0], e1 = sv[i][1]; s1 += __uint_as_float(e0); s2 += __uint_as_float(e1); }
      }
      SCHED_FENCE();
    }
  }
  s1 = half_swap_sum(s1); s2 = half_swap_sum(s2);
  const float mean = s1 * (1.0f / D);
  const float rstd = 1.0f / sqrtf(fmaxf(s2 * (1.0f / D) - mean * mean, 0.f) + LN_EPS);
  const float mr = mean * rstd;
  const float* cst = (const float*)(smem + L_CS);
  const unsigned dst = WS_M + (((tb >> 1) * 112 + 7 * j) * 2 + (tb & 1)) * 1024;
#pragma unroll
  for (int t = 0; t < 7; ++t) {
    float v[8];
#pragma unroll
    for (int g = 0; g < 2; ++g) {
      const f32x4 cg = *(const f32x4*)(cst + 32 * t + 8 * g + 4 * h), cu = *(const f32x4*)(cst + 32 * t + 16 + 8 * g + 4 * h);
#pragma unroll
      for (int i = 0; i < 4; ++i) {
        const float gv = acc[t][4 * g + i] * rstd - mr * cg[i], uv = acc[t][8 + 4 * g + i] * rstd - mr * cu[i];
        v[4 * g + i] = fmaxf(gv, 0.f) * uv;
      }
    }
    st16_dev(rws, lane * 16, dst + t * 2048, as_u4(pack8(v[0], v[1], v[2], v[3], v[4], v[5], v[6], v[7])));
  }
  WAIT_VM(0); BARRIER();
  signal_panel(ws, 2, p);
}

#define XD5 8
constexpr int P5_SLOT = 32768, P5_XD = XD5;
DI constexpr int p5_slot(int it) { return ((it + 3) & 3) * P5_SLOT; }

DI int p5_rot(int it, int rot) { const int v = it + rot; return v >= 7 ? v - 7 : v; }

DI void down_compute(char* smem, int w, int lane, char* ws, rsrc_t rws, char* out, int tp, int c, int rot) {
  const int r = lane & 31, h = lane >> 5, tt = w & 1, kh = w >> 1;
  const int tb = 2 * tp + tt, ntile = 2 * c + kh;
  u32x4 rh[2];
#pragma unroll
  for (int s = 0; s < 2; ++s) rh[s] = ld16_dev(rws, lane * 16, WS_H + (tb * 32 + 2 * ntile + s) * 1024);
  Polls pl;
  wait_panel(ws, 2, tp >> 1, (tp & 1) * 8 + c, w, pl);
  BARRIER();
  u32x4 xf[56];
  unsigned mb[7];
#pragma unroll
  for (int it = 0; it < 7; ++it) mb[it] = WS_M + ((tp * 112 + 56 * kh + 8 * p5_rot(it, rot)) * 2 + tt) * 1024;
  auto ldx = [&](int g) { xf[g] = ld16_dev(rws, lane * 16, mb[g >> 3] + (g & 7) * 2048); };
#pragma unroll
  for (int g = 0; g < P5_XD; ++g) ldx(g);
  const unsigned lb = lds_addr(smem) + kh * 16384;
  unsigned a0[2], a1[2];
#pragma unroll
  for (int b = 0; b < 2; ++b) { a0[b] = opaque(lb + wtr_off(lane, 0) + b * 65536); a1[b] = opaque(lb + wtr_off(lane, 1) + b * 65536); }
  bf16x8 wf[3][2];
  auto rd = [&](int g) {
    const int o = p5_slot(g >> 3) + (g & 7) * 2048, b = o >> 16, oo = o & 65535;
    wf[g % 3][0] = tr_frag_a(a0[b] + oo, a0[b] + oo + 1024);
    wf[g % 3][1] = tr_frag_a(a1[b] + oo, a1[b] + oo + 1024);
  };
  f32x16 acc[2] = {};
  rd(0); rd(1);
  SCHED_FENCE();
#pragma unroll
  for (int g = 0; g < 56; ++g) {
    const int n = g + 2;
    acc[0] = MFMA32(wf[g % 3][0], as_frag(xf[g]), acc[0]); SCHED_FENCE();
    if (g == 0) retire(pl);
    if (n < 56) {
      if ((n & 7) == 0) { WAIT_LGKM0(); BARRIER(); }
      rd(n);
    }
    if (g + P5_XD < 56) ldx(g + P5_XD);
    SCHED_FENCE();
    acc[1] = MFMA32(wf[g % 3][1], as_frag(xf[g]), acc[1]); SCHED_FENCE();
  }
  f32x16 a = pair_reduce<2>(smem, w, lane, acc);
  unsigned pk[8];
#pragma unroll
  for (int s = 0; s < 2; ++s) {
#pragma unroll
    for (int i = 0; i < 4; ++i) { a[8 * s + 2 * i] += bf_lo(rh[s][i]); a[8 * s + 2 * i + 1] += bf_hi(rh[s][i]); }
    const u32x4 u = as_u4(pack8(a[8 * s], a[8 * s + 1], a[8 * s + 2], a[8 * s + 3], a[8 * s + 4], a[8 * s + 5], a[8 * s + 6], a[8 * s + 7]));
#pragma unroll
    for (int i = 0; i < 4; ++i) pk[4 * s + i] = u[i];
  }
  char* dst = out + (size_t)(32 * tb + r) * 1024 + (32 * ntile + 8 * h) * 2;
#pragma unroll
  for (int s = 0; s < 2; ++s) {
    auto q0 = __builtin_amdgcn_permlane32_swap(pk[4 * s], pk[4 * s + 2], false, false);
    auto q1 = __builtin_amdgcn_permlane32_swap(pk[4 * s + 1], pk[4 * s + 3], false, false);
    *(u32x4*)(dst + s * 32) = u32x4{q0[0], q1[0], q0[1], q1[1]};
  }
}

DI void loader_345(char* smem, int lw, int lane, char* ws, rsrc_t rws, const char* wd, int j, int c, int rot) {
  f32x4 cv[8];
  const int cl = lane < 56 ? lane : 55;
#pragma unroll
  for (int i = 0; i < 8; ++i) cv[i] = *(const f32x4*)(ws + WS_CS + ((size_t)(8 * lw + i) * 112 + 7 * j) * 128 + cl * 16);
  SCHED_FENCE();
  auto wgu = [&](int s) {
#pragma unroll
    for (int i = 0; i < 7; ++i) blds16(rws, lane * 16, WS_WGU + (j * 32 * 7 + s * 28 + lw * 7 + i) * 1024, smem + (s % 5) * P4_SLOT + (lw * 7 + i) * 1024);
  };
  const rsrc_t rwd = make_rsrc(wd, F * D * 2);
  const unsigned vo = wrow_voff(lane);
  auto wdn = [&](int it) {
#pragma unroll
    for (int i = 0; i < 8; ++i) {
      const int pc = lw * 8 + i, kh = pc >> 4, rr = (pc & 15) * 8;
      blds16(rwd, vo, (896 * kh + 128 * p5_rot(it, rot) + rr) * 1024 + c * 128, smem + p5_slot(it) + pc * 1024);
    }
  };
  wgu(0); wgu(1);
  BARRIER();
  BARRIER();
  wgu(2); wgu(3); wgu(4);
  {
    f32x4 a = cv[0];
#pragma unroll
    for (int i = 1; i < 8; ++i) a += cv[i];
    if (lane < 56) *(f32x4*)(smem + L_CS + lw * 896 + lane * 16) = a;
  }
  WAIT_LGKM0(); BARRIER();
  if (lw == 0 && lane < 56) {
    const char* t = smem + L_CS + lane * 16;
    *(f32x4*)(smem + L_CS + lane * 16) = *(const f32x4*)t + *(const f32x4*)(t + 896) + *(const f32x4*)(t + 1792) + *(const f32x4*)(t + 2688);
  }
  WAIT_VM(28); WAIT_LGKM0(); BARRIER();
  WAIT_VM(21); BARRIER(); wgu(5);
  WAIT_VM(21); BARRIER(); wgu(6);
  WAIT_VM(21); BARRIER(); wgu(7);
  WAIT_VM(21); BARRIER();
  WAIT_VM(14); BARRIER(); wdn(0);
  WAIT_VM(15); BARRIER();
  WAIT_VM(8); BARRIER(); wdn(1);
  BARRIER(); wdn(2); wdn(3);
  BARRIER();
  WAIT_VM(24); BARRIER();
  WAIT_VM(16); BARRIER(); wdn(4);
  WAIT_VM(16); BARRIER(); wdn(5);
  WAIT_VM(16); BARRIER(); wdn(6);
  WAIT_VM(16); BARRIER();
  WAIT_VM(8); BARRIER();
  WAIT_VM(0); BARRIER();
  BARRIER();
}

extern "C" __global__ __launch_bounds__(512, 2) void k_main(char* __restrict__ ws, const char* __restrict__ x, const char* __restrict__ wo,
                                                 const char* __restrict__ wd, char* __restrict__ out) {
  extern __shared__ __attribute__((aligned(16))) char smem[];
  const int w = __builtin_amdgcn_readfirstlane(threadIdx.x >> 6), lane = threadIdx.x & 63;
  const rsrc_t rws = make_rsrc(ws, WS_BYTES);
  phase_attn(smem, w, lane, ws, rws, wo);
  const int xcd = blockIdx.x & 7, i = blockIdx.x >> 3;
  const int tp = 4 * xcd + (i & 3), c = i >> 2;
  const int p4 = 8 * (xcd & 1) + (i & 7), j4 = 4 * (xcd >> 1) + (i >> 3);
  const int rot = (c + 2 * (i & 3)) % 7;
  if (w >= 4) loader_345(smem, w - 4, lane, ws, rws, wd, j4, c, rot);
  else {
    oproj_compute(smem, w, lane, ws, rws, x, tp, c);
    gateup_compute(smem, w, lane, ws, rws, p4, j4);
    down_compute(smem, w, lane, ws, rws, out, tp, c, rot);
  }
}

extern "C" void kernel_launch(void* const* d_in, const int* in_sizes, int n_in, void* d_out, int out_size, void* d_ws, size_t ws_size,
                              hipStream_t stream) {
  const char* x = (const char*)d_in[0];
  const char* wqkv = (const char*)d_in[1];
  const char* wo = (const char*)d_in[2];
  const char* wg = (const char*)d_in[4];
  const char* wu = (const char*)d_in[5];
  const char* wd = (const char*)d_in[6];
  char* ws = (char*)d_ws;
  (void)hipFuncSetAttribute((const void*)k_qkv, hipFuncAttributeMaxDynamicSharedMemorySize, P1_LDS);
  (void)hipFuncSetAttribute((const void*)k_main, hipFuncAttributeMaxDynamicSharedMemorySize, MAIN_LDS);
  k_qkv<<<256, 512, P1_LDS, stream>>>(x, wqkv, wg, wu, ws, wo, wd, (const char*)d_out);
  k_main<<<256, 512, MAIN_LDS, stream>>>(ws, x, wo, wd, (char*)d_out);
}
```

```cpp
#include <hip/hip_runtime.h>
#include <hip/hip_bf16.h>
#include <stdint.h>

typedef __attribute__((ext_vector_type(8))) __bf16 bf16x8;
typedef __attribute__((ext_vector_type(2))) __bf16 bf16x2;
typedef __attribute__((ext_vector_type(16))) float f32x16;
typedef __attribute__((ext_vector_type(8))) float f32x8;
typedef __attribute__((ext_vector_type(4))) float f32x4;
typedef __attribute__((ext_vector_type(2))) float f32x2;
typedef __attribute__((ext_vector_type(4))) unsigned u32x4;
typedef __attribute__((ext_vector_type(2))) unsigned u32x2;
typedef __attribute__((ext_vector_type(4))) short s16x4;
typedef __attribute__((address_space(3))) s16x4* lds_s16x4;
typedef __attribute__((address_space(3))) void* lds_vp;
typedef const __attribute__((address_space(1))) void* g_vp;

#define POLL_GAP "4"
#define POLL_SLEEP ""
#define DI __device__ __forceinline__
#define MFMA32(a, b, c) __builtin_amdgcn_mfma_f32_32x32x16_bf16((a), (b), (c), 0, 0, 0)
#define BARRIER() asm volatile("s_barrier" ::: "memory")
#define WAIT_VM(n) asm volatile("s_waitcnt vmcnt(" #n ")" ::: "memory")
#define WAIT_LGKM0() asm volatile("s_waitcnt lgkmcnt(0)" ::: "memory")
#define SCHED_FENCE() __builtin_amdgcn_sched_barrier(0)

constexpr int S = 2048, D = 512, F = 1792;
constexpr float LN_EPS = 1e-5f;
constexpr float Q_SCALE = 0.25f * 1.4426950408889634f;

constexpr unsigned WS_Q = 0;
constexpr unsigned WS_K = WS_Q + (2u << 20);
constexpr unsigned WS_V = WS_K + (2u << 20);
constexpr unsigned WS_O = WS_V + (2u << 20);
constexpr unsigned WS_H = WS_O + (2u << 20);
constexpr unsigned WS_ST = WS_H + (2u << 20);
constexpr unsigned WS_M = WS_ST + (256u << 10);
constexpr unsigned WS_WGU = WS_M + (7u << 20);
constexpr unsigned WS_CS = WS_WGU + (3584u << 10);
constexpr unsigned WS_CNT = WS_CS + (448u << 10);
constexpr unsigned WS_BYTES = WS_CNT + 768 * 128;

typedef __amdgpu_buffer_rsrc_t rsrc_t;
typedef __attribute__((address_space(3))) const bf16x8* lds_fp;
typedef __attribute__((address_space(1))) unsigned gu32;
constexpr int SC1 = 16;
constexpr unsigned QKV_CODE_BYTES = 35840, MAIN_CODE_BYTES = 26112;

DI rsrc_t make_rsrc(const void* p, unsigned bytes) { return __builtin_amdgcn_make_buffer_rsrc((void*)p, 0, bytes, 0x00020000); }
DI void glds16(const void* g, void* l) { __builtin_amdgcn_global_load_lds((g_vp)g, (lds_vp)l, 16, 0, 0); }
DI void blds16(rsrc_t rs, unsigned voff, unsigned soff, char* l) { __builtin_amdgcn_raw_ptr_buffer_load_lds(rs, (lds_vp)l, 16, voff, soff, 0, 0); }
DI u32x4 ld16_dev(rsrc_t rs, unsigned voff, unsigned soff) { return __builtin_amdgcn_raw_buffer_load_b128(rs, voff, soff, SC1); }
DI void st16_dev(rsrc_t rs, unsigned voff, unsigned soff, u32x4 v) { __builtin_amdgcn_raw_buffer_store_b128(v, rs, voff + soff, 0, SC1); }
#define P1_AUXBITS 16
DI void st16_p1(rsrc_t rs, unsigned voff, unsigned soff, u32x4 v) { __builtin_amdgcn_raw_buffer_store_b128(v, rs, voff + soff, 0, P1_AUXBITS); }
DI bf16x8 lds_frag(const char* p) { return *(const bf16x8*)p; }
DI bf16x8 lds_frag_a(unsigned a) { return *(lds_fp)(size_t)a; }
DI unsigned lds_addr(const char* p) { return (unsigned)(size_t)(const __attribute__((address_space(3))) char*)p; }
DI unsigned touch(const char* g) { unsigned v; asm volatile("global_load_dword %0, %1, off" : "=v"(v) : "v"(g)); return v; }
#define KERNEL_ADDR(dst, sym) do { unsigned lo_, hi_; \
  asm volatile("s_getpc_b64 s[40:41]\n\ts_add_u32 s40, s40, " #sym "@rel32@lo+4\n\ts_addc_u32 s41, s41, " #sym "@rel32@hi+12\n\ts_mov_b32 %0, s40\n\ts_mov_b32 %1, s41" \
               : "=s"(lo_), "=s"(hi_) :: "s40", "s41", "scc"); \
  dst = ((unsigned long long)hi_ << 32) | lo_; } while (0)
DI void dma_piece(u32x4 rs, unsigned voff, unsigned soff, unsigned lds) {
  asm volatile("s_mov_b32 m0, %0\n\ts_nop 0\n\tbuffer_load_dwordx4 %1, %2, %3 offen lds" :: "s"(lds), "v"(voff), "s"(rs), "s"(soff));
}
DI unsigned opaque(unsigned v) { asm volatile("" : "+v"(v)); return v; }
DI bf16x8 tr_frag(const char* p0, const char* p1) {
  s16x4 lo = __builtin_amdgcn_ds_read_tr16_b64_v4i16((lds_s16x4)p0);
  s16x4 hi = __builtin_amdgcn_ds_read_tr16_b64_v4i16((lds_s16x4)p1);
  return __builtin_bit_cast(bf16x8, __builtin_shufflevector(lo, hi, 0, 1, 2, 3, 4, 5, 6, 7));
}
DI bf16x8 tr_frag_a(unsigned a0, unsigned a1) {
  s16x4 lo = __builtin_amdgcn_ds_read_tr16_b64_v4i16((lds_s16x4)(size_t)a0);
  s16x4 hi = __builtin_amdgcn_ds_read_tr16_b64_v4i16((lds_s16x4)(size_t)a1);
  return __builtin_bit_cast(bf16x8, __builtin_shufflevector(lo, hi, 0, 1, 2, 3, 4, 5, 6, 7));
}
DI bf16x8 pack8(float a0, float a1, float a2, float a3, float a4, float a5, float a6, float a7) {
  f32x8 v = {a0, a1, a2, a3, a4, a5, a6, a7};
  return __builtin_convertvector(v, bf16x8);
}
DI u32x4 as_u4(bf16x8 v) { return __builtin_bit_cast(u32x4, v); }
DI bf16x8 as_frag(u32x4 v) { return __builtin_bit_cast(bf16x8, v); }
DI float bf_lo(unsigned u) { return __uint_as_float(u << 16); }
DI float bf_hi(unsigned u) { return __uint_as_float(u & 0xffff0000u); }
DI float half_swap_sum(float v) {
  auto r = __builtin_amdgcn_permlane32_swap(__float_as_uint(v), __float_as_uint(v), false, false);
  return __uint_as_float(r[0]) + __uint_as_float(r[1]);
}
DI float half_swap_max(float v) {
  auto r = __builtin_amdgcn_permlane32_swap(__float_as_uint(v), __float_as_uint(v), false, false);
  return fmaxf(__uint_as_float(r[0]), __uint_as_float(r[1]));
}
DI void add_f32(float& a, float v) { asm("v_add_f32 %0, %0, %1" : "+v"(a) : "v"(v)); }
DI void fma_sq(float& a, float v) { asm("v_fma_f32 %0, %1, %1, %0" : "+v"(a) : "v"(v)); }
DI float colsum_frag(bf16x8 f, float acc) {
  const bf16x2 ones = {(__bf16)1.0f, (__bf16)1.0f};
  const u32x4 u = as_u4(f);
#pragma unroll
  for (int i = 0; i < 4; ++i) { const unsigned e = u[i]; acc = __builtin_amdgcn_fdot2_f32_bf16(__builtin_bit_cast(bf16x2, e), ones, acc, false); }
  return acc;
}

DI void signal_panel(char* ws, int phase, int panel) {
  if (threadIdx.x < 16)
    __hip_atomic_fetch_add((gu32*)(ws + WS_CNT + ((phase * 16 + panel) * 16 + threadIdx.x) * 128), 1u, __ATOMIC_RELAXED, __HIP_MEMORY_SCOPE_AGENT);
}
struct Polls { unsigned v[4]; };
DI void wait_panel(char* ws, int phase, int panel, int replica, int w, Polls& p) {
  const char* word = ws + WS_CNT + ((phase * 16 + panel) * 16 + replica) * 128;
  unsigned left = 1u << 17, seen;
  unsigned zero = 0;
  asm volatile(
      "s_cmp_lg_u32 %8, 0\n\ts_cbranch_scc1 2f\n"
      "1:\n\tglobal_load_dword %0, %6, %7 sc1\n\ts_waitcnt vmcnt(0)\n\tv_readfirstlane_b32 %5, %0\n\ts_cmp_ge_u32 %5, 16\n\ts_cbranch_scc1 2f\n\t" POLL_SLEEP
      "s_sub_u32 %4, %4, 1\n\ts_cmp_lg_u32 %4, 0\n\ts_cbranch_scc1 1b\n"
      "2:"
      : "=&v"(p.v[0]), "=&v"(p.v[1]), "=&v"(p.v[2]), "=&v"(p.v[3]), "+s"(left), "=&s"(seen)
      : "v"(zero), "s"(word), "s"(w)
      : "scc", "memory");
  return;
#define POLL_STEP(k) "s_waitcnt vmcnt(3)\n\tv_readfirstlane_b32 %5, %" #k "\n\ts_cmp_ge_u32 %5, 16\n\ts_cbranch_scc1 2f\n\tglobal_load_dword %" #k ", %6, %7 sc1\n\t"
  asm volatile(
      "s_cmp_lg_u32 %8, 0\n\ts_cbranch_scc1 2f\n\t"
      "global_load_dword %0, %6, %7 sc1\n\ts_sleep " POLL_GAP "\n\t"
      "global_load_dword %1, %6, %7 sc1\n\ts_sleep " POLL_GAP "\n\t"
      "global_load_dword %2, %6, %7 sc1\n\ts_sleep " POLL_GAP "\n\t"
      "global_load_dword %3, %6, %7 sc1\n"
      "1:\n\t" POLL_STEP(0) POLL_STEP(1) POLL_STEP(2) POLL_STEP(3)
      "s_sub_u32 %4, %4, 1\n\ts_cmp_lg_u32 %4, 0\n\ts_cbranch_scc1 1b\n"
      "2:"
      : "=&v"(p.v[0]), "=&v"(p.v[1]), "=&v"(p.v[2]), "=&v"(p.v[3]), "+s"(left), "=&s"(seen)
      : "v"(zero), "s"(word), "s"(w)
      : "scc", "memory");
#undef POLL_STEP
}
DI void retire(const Polls& p) { asm volatile("; retire %0 %1 %2 %3" :: "v"(p.v[0]), "v"(p.v[1]), "v"(p.v[2]), "v"(p.v[3])); }
constexpr int P1_SLOT = 28672, P1_NSLOT = 5, P1_AUX = P1_SLOT * P1_NSLOT, P1_PREP = P1_AUX + 4096;
constexpr int P1_LDS = P1_PREP + 16384;

template <int VMASK>
DI void p1_compute(char* smem, int w, int lane, int p, int j, const char* wqkv, char* ws, const char* wo, const char* wd, const char* out) {
  const int r = lane & 31, h = lane >> 5;
  const int row = 32 * w + r, sw = (r >> 1) & 7;
  const unsigned lb = lds_addr(smem);
  unsigned xa[4][3], wa[3];
#pragma unroll
  for (int ks = 0; ks < 4; ++ks) {
    const unsigned o = lb + row * 128 + (((2 * ks + h) ^ sw) << 4);
#pragma unroll
    for (int b = 0; b < 3; ++b) xa[ks][b] = opaque(o + b * 2 * P1_SLOT);
  }
  {
    const int q = (lane & 15) >> 2, pp = lane & 3, blk = (lane >> 4) & 1;
    const unsigned o = lb + 16384 + (8 * h + q) * 192 + (2 * blk + (pp >> 1)) * 16 + 8 * (pp & 1);
#pragma unroll
    for (int b = 0; b < 3; ++b) wa[b] = opaque(o + b * 2 * P1_SLOT);
  }
  f32x16 acc[3] = {};
  float cs[3] = {0.f, 0.f, 0.f};
  bf16x8 xf[3], wf[3][3];
  auto rdx = [&](int g) { const int sl = (g >> 2) % P1_NSLOT; xf[g % 3] = lds_frag_a(xa[g & 3][sl >> 1] + (sl & 1) * P1_SLOT); };
  auto rdw = [&](int g, int c) {
    const int sl = (g >> 2) % P1_NSLOT;
    const unsigned a = wa[sl >> 1] + (sl & 1) * P1_SLOT + (16 * (g & 3)) * 192 + c * 64;
    wf[g % 3][c] = tr_frag_a(a, a + 4 * 192);
  };
  auto mm = [&](int g, int c) {
    if ((VMASK >> c) & 1) acc[c] = MFMA32(xf[g % 3], wf[g % 3][c], acc[c]);
    else acc[c] = MFMA32(wf[g % 3][c], xf[g % 3], acc[c]);
  };
  const unsigned long long wa64 = (unsigned long long)wqkv;
  const u32x4 rw = {(unsigned)wa64, (unsigned)(wa64 >> 32) & 0xffffu, (unsigned)(D * 3 * D * 2), 0x00020000u};
  unsigned wo_[3];
#pragma unroll
  for (int i = 0; i < 3; ++i) { const int g = 64 * (3 * w + i) + lane; wo_[i] = (g / 12) * 3072 + 192 * j + (g % 12) * 16; }
  auto wpiece = [&](int s, int i) { dma_piece(rw, wo_[i], s * 64 * 3072, lb + (s % P1_NSLOT) * P1_SLOT + 16384 + (3 * w + i) * 1024); };
#pragma unroll
  for (int s = 0; s < 3; ++s) { wpiece(s, 0); wpiece(s, 1); wpiece(s, 2); }
  unsigned warm;
  {
    unsigned long long ka, kb;
    KERNEL_ADDR(ka, k_qkv); KERNEL_ADDR(kb, k_main);
    const unsigned i = blockIdx.x >> 3, v = 4 * w + (lane & 3);
    const char* src = v < 9 ? (const char*)ka + min((i * 9 + v) * 128u, QKV_CODE_BYTES - 128u) : (const char*)kb + min((i * 7 + v - 9) * 128u, MAIN_CODE_BYTES - 128u);
    asm volatile("global_load_dword %0, %1, off" : "=v"(warm) : "v"(src));
  }
  unsigned warm2 = 0;
  WAIT_VM(7); BARRIER();
#pragma unroll
  for (int g = 0; g < 2; ++g) { rdx(g); rdw(g, 0); rdw(g, 1); rdw(g, 2); }
  SCHED_FENCE();
#pragma unroll
  for (int g = 0; g < 32; ++g) {
    const int n = g + 2;
    mm(g, 0); SCHED_FENCE();
    if (n < 32) {
      if ((n & 3) == 0) {
        if (n <= 8) WAIT_VM(7); else if (n <= 20) WAIT_VM(6); else if (n == 24) WAIT_VM(3); else WAIT_VM(0);
        WAIT_LGKM0(); BARRIER();
      }
      rdx(n); rdw(n, 0); SCHED_FENCE();
    }
    mm(g, 1); SCHED_FENCE();
    {
      const int m = g < 2 ? g : g + 2;
      const int s = g < 2 ? 3 : (m >> 2) + 3, i = g < 2 ? g : (m & 3);
      if (s < 8 && i < 3) { wpiece(s, i); SCHED_FENCE(); }
      if (g == 1) { wpiece(3, 2); SCHED_FENCE(); }
    }
    if (n < 32) { rdw(n, 1); rdw(n, 2); SCHED_FENCE(); }
    mm(g, 2); SCHED_FENCE();
    if ((g & 3) == w) {
#pragma unroll
      for (int c = 0; c < 3; ++c) cs[c] = colsum_frag(wf[g % 3][c], cs[c]);
    }
    SCHED_FENCE();
  }
  asm volatile("; warm %0" :: "v"(warm));
  float* csp = (float*)(smem + P1_AUX);
  float* cst = csp + 384;
  f32x2* st = (f32x2*)(smem + P1_AUX + 2048);
#pragma unroll
  for (int c = 0; c < 3; ++c) { float t = half_swap_sum(cs[c]); if (h == 0) csp[w * 96 + c * 32 + r] = t; }
  WAIT_LGKM0(); BARRIER();
  if (w < 2) { int t = w * 64 + lane; if (t < 96) cst[t] = csp[t] + csp[96 + t] + csp[192 + t] + csp[288 + t]; }
  WAIT_LGKM0(); BARRIER();
  const int tb = 4 * p + w;
  const rsrc_t rws = make_rsrc(ws, WS_BYTES);
  const f32x2 mine = st[row];
#pragma unroll
  for (int c = 0; c < 3; ++c) {
    const int c0 = 96 * j + 32 * c;
    if ((VMASK >> c) & 1) {
      const float csv = cst[32 * c + r];
      float v[16];
#pragma unroll
      for (int i = 0; i < 16; ++i) { f32x2 t = st[32 * w + (i & 3) + 8 * (i >> 2) + 4 * h]; v[i] = acc[c][i] * t[0] - t[1] * csv; }
      const unsigned dst = WS_V + (((c0 - 1024) >> 5) * 128 + 2 * tb) * 1024;
      st16_p1(rws, lane * 16, dst, as_u4(pack8(v[0], v[1], v[2], v[3], v[4], v[5], v[6], v[7])));
      st16_p1(rws, lane * 16, dst + 1024, as_u4(pack8(v[8], v[9], v[10], v[11], v[12], v[13], v[14], v[15])));
    } else {
      const bool isq = c0 < 512;
      const float sc = isq ? Q_SCALE : 1.0f;
      const float a = mine[0] * sc, b = mine[1] * sc;
      float v[16];
#pragma unroll
      for (int g = 0; g < 4; ++g) {
        f32x4 cv = *(const f32x4*)(cst + 32 * c + 8 * g + 4 * h);
#pragma unroll
        for (int i = 0; i < 4; ++i) v[4 * g + i] = acc[c][4 * g + i] * a - b * cv[i];
      }
      const int head = (c0 & 511) >> 4;
      const unsigned dst = (isq ? WS_Q : WS_K) + (head * 64 + tb) * 1024;
      st16_p1(rws, lane * 16, dst, as_u4(pack8(v[0], v[1], v[2], v[3], v[4], v[5], v[6], v[7])));
      st16_p1(rws, lane * 16, dst + 65536, as_u4(pack8(v[8], v[9], v[10], v[11], v[12], v[13], v[14], v[15])));
    }
  }
  asm volatile("; warm %0" :: "v"(warm2));
}

DI void p1_helper(char* smem, int lw, int lane, int p, int j, const char* x, const char* wg, const char* wu, char* ws) {
  const rsrc_t rx = make_rsrc(x, S * D * 2);
  unsigned xo[4];
#pragma unroll
  for (int i = 0; i < 4; ++i) {
    int row = 32 * lw + 8 * i + (lane >> 3);
    xo[i] = (128 * p + row) * 1024 + (((lane & 7) ^ ((row >> 1) & 7)) << 4);
  }
  auto issue = [&](int s) {
    char* sl = smem + (s % P1_NSLOT) * P1_SLOT;
#pragma unroll
    for (int i = 0; i < 4; ++i) blds16(rx, xo[i], s * 128, sl + (4 * lw + i) * 1024);
  };
  issue(0); issue(1); issue(2);
  const int b = blockIdx.x, cg = b % 28, pks = 4 * (b / 28) + lw;
  const bool prep = b < 224;
  char* stg = smem + P1_PREP + lw * 4096;
  auto prep_issue = [&]() {
    const rsrc_t rg = make_rsrc(wg, D * F * 2), ru = make_rsrc(wu, D * F * 2);
    const unsigned off = (16 * pks + (lane >> 3)) * 3584 + cg * 128 + (lane & 7) * 16;
    blds16(rg, off, 0, stg); blds16(rg, off, 8 * 3584, stg + 1024);
    blds16(ru, off, 0, stg + 2048); blds16(ru, off, 8 * 3584, stg + 3072);
  };
  const int r = lane & 31, h = lane >> 5, row = 32 * lw + r, sw = (r >> 1) & 7;
  bf16x8 pf_[4];
  auto prep_read = [&]() {
    const int blk = (lane >> 4) & 1, q = (lane & 15) >> 2, pp = lane & 3;
    const unsigned bs = lds_addr(stg) + blk * 2048 + (4 * h + q) * 128 + (pp >> 1) * 16 + 8 * (pp & 1);
    u32x2 t[8];
#define PREP_RD(i, off) asm volatile("ds_read_b64_tr_b16 %0, %1 offset:" #off : "=v"(t[i]) : "v"(bs))
    PREP_RD(0, 0); PREP_RD(1, 1024); PREP_RD(2, 32); PREP_RD(3, 1056); PREP_RD(4, 64); PREP_RD(5, 1088); PREP_RD(6, 96); PREP_RD(7, 1120);
#undef PREP_RD
    asm volatile("s_waitcnt lgkmcnt(0)" : "+v"(t[0]), "+v"(t[1]), "+v"(t[2]), "+v"(t[3]), "+v"(t[4]), "+v"(t[5]), "+v"(t[6]), "+v"(t[7]));
#pragma unroll
    for (int i = 0; i < 4; ++i) { const u32x2 lo = t[2 * i], hi = t[2 * i + 1]; pf_[i] = as_frag(u32x4{lo[0], lo[1], hi[0], hi[1]}); }
  };
  auto prep_frag = [&](int i) {
    const int fs = 4 * cg + i, jj = fs / 7, t = fs % 7;
    st16_p1(make_rsrc(ws, WS_BYTES), lane * 16, WS_WGU + ((jj * 32 + pks) * 7 + t) * 1024, as_u4(pf_[i]));
    const float c = half_swap_sum(colsum_frag(pf_[i], 0.f));
    if (h == 0) *(float*)(ws + WS_CS + ((size_t)pks * 112 + fs) * 128 + r * 4) = c;
  };
  float sum0 = 0.f, sum1 = 0.f, sq0 = 0.f, sq1 = 0.f;
  auto piece = [&](int s, int i) { blds16(rx, xo[i], s * 128, smem + (s % P1_NSLOT) * P1_SLOT + (4 * lw + i) * 1024); };
  auto step = [&](int s, int n) {
    const char* sl = smem + (s % P1_NSLOT) * P1_SLOT + row * 128;
    u32x4 u[4];
#pragma unroll
    for (int c = 0; c < 4; ++c) u[c] = *(const u32x4*)(sl + (((4 * h + c) ^ sw) << 4));
    SCHED_FENCE();
#pragma unroll
    for (int c = 0; c < 4; ++c) {
      if (n < 8) piece(n, c);
      SCHED_FENCE();
#pragma unroll
      for (int i = 0; i < 4; ++i) {
        const float lo = bf_lo(u[c][i]), hi = bf_hi(u[c][i]);
        add_f32(sum0, lo); add_f32(sum1, hi); fma_sq(sq0, lo); fma_sq(sq1, hi);
      }
      SCHED_FENCE();
    }
  };
  if (prep) {
    prep_issue();
    WAIT_VM(12); BARRIER();
    step(0, 3); WAIT_VM(12); WAIT_LGKM0(); BARRIER();
    step(1, 4); WAIT_VM(12); WAIT_LGKM0(); BARRIER();
  } else {
    WAIT_VM(8); BARRIER();
    step(0, 3); WAIT_VM(8); WAIT_LGKM0(); BARRIER();
    step(1, 4); WAIT_VM(8); WAIT_LGKM0(); BARRIER();
  }
  step(2, 5); WAIT_VM(8); WAIT_LGKM0(); BARRIER();
  step(3, 6); WAIT_VM(8); WAIT_LGKM0(); BARRIER();
  step(4, 7); WAIT_VM(8); WAIT_LGKM0(); BARRIER();
  step(5, 8); WAIT_VM(4);
  if (prep) prep_read();
  WAIT_LGKM0(); BARRIER();
  step(6, 8); WAIT_VM(0);
  if (prep) prep_frag(0);
  WAIT_LGKM0(); BARRIER();
  step(7, 8);
  const float s1 = half_swap_sum(sum0 + sum1), s2 = half_swap_sum(sq0 + sq1);
  const float mean = s1 * (1.0f / D);
  const float var = fmaxf(s2 * (1.0f / D) - mean * mean, 0.f);
  const float rstd = 1.0f / sqrtf(var + LN_EPS);
  f32x2* st = (f32x2*)(smem + P1_AUX + 2048);
  if (h == 0) st[row] = f32x2{rstd, mean * rstd};
  WAIT_LGKM0(); BARRIER();
  BARRIER();
  if (prep) prep_frag(1);
  if (prep) { prep_frag(2); prep_frag(3); }
}

extern "C" __global__ __launch_bounds__(512, 2) void k_qkv(const char* __restrict__ x, const char* __restrict__ wqkv, const char* __restrict__ wg,
                                                const char* __restrict__ wu, char* __restrict__ ws,
                                                const char* __restrict__ wo, const char* __restrict__ wd, const char* __restrict__ out) {
  extern __shared__ __attribute__((aligned(16))) char smem[];
  const int w = __builtin_amdgcn_readfirstlane(threadIdx.x >> 6), lane = threadIdx.x & 63;
  const int xcd = blockIdx.x & 7, i = blockIdx.x >> 3;
  const int p = 4 * (xcd & 3) + (i & 3), j = 8 * (xcd >> 2) + (i >> 2);
  if (threadIdx.x < 3) *(unsigned*)(ws + WS_CNT + (blockIdx.x * 3 + threadIdx.x) * 128) = 0u;
  if (w >= 4) p1_helper(smem, w - 4, lane, p, j, x, wg, wu, ws);
  else if (j <= 9) p1_compute<0>(smem, w, lane, p, j, wqkv, ws, wo, wd, out);
  else if (j == 10) p1_compute<4>(smem, w, lane, p, j, wqkv, ws, wo, wd, out);
  else p1_compute<7>(smem, w, lane, p, j, wqkv, ws, wo, wd, out);
}

constexpr int P2_K = 0, P2_V = 24576, P2_VP = 1040, P2_Q = P2_V + 24 * P2_VP;
constexpr int L_WO = 77824;
constexpr int L_RED = 143360;
constexpr int L_CS = L_RED + 16384;
constexpr int MAIN_LDS = 163840;

template <bool FULL>
DI void attn_task(const char* smem, int lane, int hh, int wq, int qb, rsrc_t rws, int head) {
  const int r = lane & 31, h = lane >> 5;
  const char* kb_ = smem + P2_K + (hh * 12 + wq) * 1024 + lane * 16;
  const bool valid = (r >> 4) == hh;
  const char* vb_ = smem + P2_V + 2 * wq * P2_VP + (valid ? lane * 16 : 1024);
  const bf16x8 qf = lds_frag(smem + P2_Q + (hh * 4 + wq) * 1024 + lane * 16);
  const f32x16 zero = {};
  const int t0 = FULL ? 1 : ((8 - qb) > 1 ? (8 - qb) : 1);
  const bool have_first = FULL || qb >= 8;
  f32x16 sm;
  {
    const f32x16 sd = MFMA32(lds_frag(kb_ + 8 * 1024), qf, zero);
    f32x16 sf;
    if (have_first) sf = MFMA32(lds_frag(kb_), qf, zero);
    else { for (int i = 0; i < 16; ++i) sf[i] = -1e30f; }
#pragma unroll
    for (int i = 0; i < 16; ++i) { const int jj = (i & 3) + 8 * (i >> 2) + 4 * h; sm[i] = jj <= r ? sd[i] : sf[i]; }
  }
  float m = sm[0];
#pragma unroll
  for (int i = 1; i < 16; ++i) m = fmaxf(m, sm[i]);
  if (FULL) {
#pragma unroll
    for (int t = 1; t < 8; ++t) {
      const f32x16 s = MFMA32(lds_frag(kb_ + t * 1024), qf, zero);
#pragma unroll
      for (int i = 0; i < 16; i += 2) m = fmaxf(fmaxf(m, s[i]), s[i + 1]);
    }
  } else {
    for (int t = t0; t < 8; ++t) {
      const f32x16 s = MFMA32(lds_frag(kb_ + t * 1024), qf, zero);
#pragma unroll
      for (int i = 0; i < 16; i += 2) m = fmaxf(fmaxf(m, s[i]), s[i + 1]);
    }
  }
  m = half_swap_max(m);
  f32x16 negm;
#pragma unroll
  for (int i = 0; i < 16; ++i) negm[i] = -m;
  f32x16 o = zero;
  {
    float pv[16];
#pragma unroll
    for (int i = 0; i < 16; ++i) pv[i] = __builtin_amdgcn_exp2f(sm[i] - m);
    u32x4 pa = __builtin_bit_cast(u32x4, pack8(pv[0], pv[1], pv[2], pv[3], pv[4], pv[5], pv[6], pv[7]));
    u32x4 pb = __builtin_bit_cast(u32x4, pack8(pv[8], pv[9], pv[10], pv[11], pv[12], pv[13], pv[14], pv[15]));
    u32x4 da, db;
#pragma unroll
    for (int i = 0; i < 4; ++i) {
      const int ja = ((2 * i) & 3) + 8 * ((2 * i) >> 2) + 4 * h;
      const unsigned ma = (ja <= r ? 0xffffu : 0u) | (ja + 1 <= r ? 0xffff0000u : 0u);
      const unsigned mb = (ja + 16 <= r ? 0xffffu : 0u) | (ja + 17 <= r ? 0xffff0000u : 0u);
      da[i] = pa[i] & ma; db[i] = pb[i] & mb;
    }
    o = MFMA32(lds_frag(vb_ + 16 * P2_VP), __builtin_bit_cast(bf16x8, da), o);
    o = MFMA32(lds_frag(vb_ + 17 * P2_VP), __builtin_bit_cast(bf16x8, db), o);
    if (have_first) {
      o = MFMA32(lds_frag(vb_), __builtin_bit_cast(bf16x8, pa ^ da), o);
      o = MFMA32(lds_frag(vb_ + P2_VP), __builtin_bit_cast(bf16x8, pb ^ db), o);
    }
  }
  auto tile = [&](int t) {
    const f32x16 s = MFMA32(lds_frag(kb_ + t * 1024), qf, negm);
    float pv[16];
#pragma unroll
    for (int i = 0; i < 16; ++i) pv[i] = __builtin_amdgcn_exp2f(s[i]);
    o = MFMA32(lds_frag(vb_ + (2 * t) * P2_VP), pack8(pv[0], pv[1], pv[2], pv[3], pv[4], pv[5], pv[6], pv[7]), o);
    o = MFMA32(lds_frag(vb_ + (2 * t + 1) * P2_VP), pack8(pv[8], pv[9], pv[10], pv[11], pv[12], pv[13], pv[14], pv[15]), o);
  };
  if (FULL) {
#pragma unroll
    for (int t = 1; t < 8; ++t) tile(t);
  } else {
    for (int t = t0; t < 8; ++t) tile(t);
  }
  const float l = hh ? o[0] : o[8];
  const float inv = 1.0f / l;
  float ov[8];
#pragma unroll
  for (int i = 0; i < 8; ++i) ov[i] = (hh ? o[8 + i] : o[i]) * inv;
  st16_dev(rws, lane * 16, WS_O + (((qb >> 1) * 32 + head) * 2 + (qb & 1)) * 1024, as_u4(pack8(ov[0], ov[1], ov[2], ov[3], ov[4], ov[5], ov[6], ov[7])));
}

DI unsigned wrow_voff(int lane) { return (lane >> 3) * 1024 + (((lane & 7) ^ (((lane >> 4) & 1) << 2)) << 4); }
DI unsigned wtr_off(int lane, int nt) {
  const int h = lane >> 5, blk = (lane >> 4) & 1, q = (lane & 15) >> 2, pp = lane & 3, sw = (q >> 1) & 1;
  return (4 * h + q) * 128 + (2 * blk + (pp >> 1) + 4 * (nt ^ sw)) * 16 + 8 * (pp & 1);
}

DI void phase_attn(char* smem, int w, int lane, char* ws, rsrc_t rws, const char* wo) {
  const int xcd = blockIdx.x & 7, i = blockIdx.x >> 3;
  const int hp = 2 * xcd + (i & 1), p = i >> 1;
  const int kb0 = 4 * p - 8;
  const int hh = w >> 2, wq = w & 3, qb = 4 * p + wq, head = 2 * hp + hh;
  blds16(rws, lane * 16, WS_Q + (head * 64 + qb) * 1024, smem + P2_Q + w * 1024);
#pragma unroll
  for (int n = 0; n < 3; ++n) {
    const int t = wq * 3 + n, sv = w * 3 + n;
    const int kt = kb0 + t < 0 ? 0 : kb0 + t, vt = 2 * kb0 + sv < 0 ? 0 : 2 * kb0 + sv;
    blds16(rws, lane * 16, WS_K + (head * 64 + kt) * 1024, smem + P2_K + (hh * 12 + t) * 1024);
    blds16(rws, lane * 16, WS_V + (hp * 128 + vt) * 1024, smem + P2_V + sv * P2_VP);
  }
  if (threadIdx.x < 24) *(u32x4*)(smem + P2_V + threadIdx.x * P2_VP + 1024) = u32x4{0x3f803f80u, 0x3f803f80u, 0x3f803f80u, 0x3f803f80u};
  WAIT_VM(0); WAIT_LGKM0(); BARRIER();
  {
    const rsrc_t rwo = make_rsrc(wo, D * D * 2);
    const int c = i >> 2;
    const unsigned vo = wrow_voff(lane);
#pragma unroll
    for (int n = 0; n < 8; ++n) blds16(rwo, vo, (8 * w + n) * 8192 + c * 128, smem + L_WO + (8 * w + n) * 1024);
  }
  if (qb >= 8) attn_task<true>(smem, lane, hh, wq, qb, rws, head);
  else attn_task<false>(smem, lane, hh, wq, qb, rws, head);
  WAIT_VM(0); WAIT_LGKM0(); BARRIER();
  signal_panel(ws, 0, p);
}

template <int NACC>
DI f32x16 pair_reduce(char* smem, int w, int lane, const f32x16 (&acc)[NACC]) {
  char* dst = smem + L_RED + w * 4096 + lane * 16;
  auto put = [&](const f32x16& send) {
#pragma unroll
    for (int g = 0; g < 4; ++g) *(f32x4*)(dst + g * 1024) = f32x4{send[4 * g], send[4 * g + 1], send[4 * g + 2], send[4 * g + 3]};
  };
  f32x16 a;
  if (w >> 1) { put(acc[0]); a = acc[1]; asm volatile("" : "+v"(a)); }
  else { put(acc[1]); a = acc[0]; asm volatile("" : "+v"(a)); }
  WAIT_LGKM0(); BARRIER();
#pragma unroll
  for (int g = 0; g < 4; ++g) {
    const f32x4 v = *(const f32x4*)(smem + L_RED + ((w ^ 2) * 4 + g) * 1024 + lane * 16);
    a[4 * g] += v[0]; a[4 * g + 1] += v[1]; a[4 * g + 2] += v[2]; a[4 * g + 3] += v[3];
  }
  return a;
}

DI void oproj_compute(char* smem, int w, int lane, char* ws, rsrc_t rws, const char* x, int tp, int c) {
  const int r = lane & 31, h = lane >> 5, tt = w & 1, kh = w >> 1;
  const int tb = 2 * tp + tt, ntile = 2 * c + kh;
  u32x2 rx[4];
#pragma unroll
  for (int g = 0; g < 4; ++g) rx[g] = *(const u32x2*)(x + (size_t)(32 * tb + r) * 1024 + (32 * ntile + 8 * g + 4 * h) * 2);
  Polls pl;
  wait_panel(ws, 0, tp >> 1, (tp & 1) * 8 + c, w, pl);
  BARRIER();
  u32x4 xf[16];
#pragma unroll
  for (int g = 0; g < 16; ++g) xf[g] = ld16_dev(rws, lane * 16, WS_O + ((tp * 32 + 16 * kh + g) * 2 + tt) * 1024);
  const unsigned lb = lds_addr(smem) + L_WO + kh * 32768;
  const unsigned a0 = opaque(lb + wtr_off(lane, 0)), a1 = opaque(lb + wtr_off(lane, 1));
  bf16x8 wf[3][2];
  auto rd = [&](int g) {
    wf[g % 3][0] = tr_frag_a(a0 + g * 2048, a0 + g * 2048 + 1024);
    wf[g % 3][1] = tr_frag_a(a1 + g * 2048, a1 + g * 2048 + 1024);
  };
  f32x16 acc[2] = {};
  rd(0); rd(1);
  SCHED_FENCE();
#pragma unroll
  for (int g = 0; g < 16; ++g) {
    acc[0] = MFMA32(wf[g % 3][0], as_frag(xf[g]), acc[0]); SCHED_FENCE();
    if (g == 0) retire(pl);
    if (g + 2 < 16) { rd(g + 2); SCHED_FENCE(); }
    acc[1] = MFMA32(wf[g % 3][1], as_frag(xf[g]), acc[1]); SCHED_FENCE();
  }
  f32x16 a = pair_reduce<2>(smem, w, lane, acc);
#pragma unroll
  for (int g = 0; g < 4; ++g) {
    a[4 * g] += bf_lo(rx[g][0]); a[4 * g + 1] += bf_hi(rx[g][0]); a[4 * g + 2] += bf_lo(rx[g][1]); a[4 * g + 3] += bf_hi(rx[g][1]);
  }
  float s1 = 0.f, s2 = 0.f;
#pragma unroll
  for (int s = 0; s < 2; ++s) {
    const u32x4 u = as_u4(pack8(a[8 * s], a[8 * s + 1], a[8 * s + 2], a[8 * s + 3], a[8 * s + 4], a[8 * s + 5], a[8 * s + 6], a[8 * s + 7]));
    st16_dev(rws, lane * 16, WS_H + (tb * 32 + 2 * ntile + s) * 1024, u);
#pragma unroll
    for (int i = 0; i < 4; ++i) { const float lo = bf_lo(u[i]), hi = bf_hi(u[i]); s1 += lo + hi; s2 += lo * lo + hi * hi; }
  }
  s1 = half_swap_sum(s1); s2 = half_swap_sum(s2);
  if (h == 0) __builtin_amdgcn_raw_buffer_store_b64(u32x2{__float_as_uint(s1), __float_as_uint(s2)}, rws, r * 8, WS_ST + (tb * 16 + ntile) * 256, SC1);
  WAIT_VM(0); BARRIER();
  signal_panel(ws, 1, tp >> 1);
}

#define XD4 12
constexpr int P4_SLOT = 28672, P4_XD = XD4;

DI void gateup_compute(char* smem, int w, int lane, char* ws, rsrc_t rws, int p, int j) {
  const int r = lane & 31, h = lane >> 5, tb = 4 * p + w;
  Polls pl;
  wait_panel(ws, 1, p, j, w, pl);
  BARRIER();
  u32x4 xf[32];
  auto ldx = [&](int g) { xf[g] = ld16_dev(rws, lane * 16, WS_H + (tb * 32 + g) * 1024); };
  ldx(0); ldx(1);
  u32x2 sv[8];
#pragma unroll
  for (int i = 0; i < 8; ++i) sv[i] = __builtin_amdgcn_raw_buffer_load_b64(rws, (8 * h + i) * 256 + r * 8, WS_ST + tb * 4096, SC1);
#pragma unroll
  for (int g = 2; g < P4_XD; ++g) ldx(g);
  const unsigned lb = lds_addr(smem) + lane * 16;
  unsigned ba[3];
#pragma unroll
  for (int b = 0; b < 3; ++b) ba[b] = opaque(lb + b * 2 * P4_SLOT);
  bf16x8 wf[2][7];
  auto rdw = [&](int g, int t) {
    const int sl = (g >> 2) % 5;
    wf[g & 1][t] = lds_frag_a(ba[sl >> 1] + (sl & 1) * P4_SLOT + ((g & 3) * 7 + t) * 1024);
  };
  f32x16 acc[7] = {};
#pragma unroll
  for (int t = 0; t < 7; ++t) rdw(0, t);
  float s1 = 0.f, s2 = 0.f;
  SCHED_FENCE();
#pragma unroll
  for (int g = 0; g < 32; ++g) {
    const int n = g + 1;
#pragma unroll
    for (int t = 0; t < 7; ++t) {
      acc[t] = MFMA32(wf[g & 1][t], as_frag(xf[g]), acc[t]); SCHED_FENCE();
      if (g == 0 && t == 0) retire(pl);
      if (t == 0 && n < 32 && (n & 3) == 0) { WAIT_LGKM0(); BARRIER(); }
      if (t == 0 && n == 32) { WAIT_LGKM0(); BARRIER(); }
      if (n < 32) rdw(n, t);
      if (t == 1 && g + P4_XD < 32) ldx(g + P4_XD);
      if (g == 2 && t == 2) {
#pragma unroll
        for (int i = 0; i < 8; ++i) { const unsigned e0 = sv[i][0], e1 = sv[i][1]; s1 += __uint_as_float(e0); s2 += __uint_as_float(e1); }
      }
      SCHED_FENCE();
    }
  }
  s1 = half_swap_sum(s1); s2 = half_swap_sum(s2);
  const float mean = s1 * (1.0f / D);
  const float rstd = 1.0f / sqrtf(fmaxf(s2 * (1.0f / D) - mean * mean, 0.f) + LN_EPS);
  const float rr = rstd * rstd;
  const f32x2 nm = {-mean, -mean}, rr2 = {rr, rr};
  const float* cst = (const float*)(smem + L_CS);
  const unsigned dst = WS_M + (((tb >> 1) * 112 + 7 * j) * 2 + (tb & 1)) * 1024;
#pragma unroll
  for (int t = 0; t < 7; ++t) {
    float v[8];
#pragma unroll
    for (int g = 0; g < 2; ++g) {
      const f32x4 cg = *(const f32x4*)(cst + 32 * t + 8 * g + 4 * h), cu = *(const f32x4*)(cst + 32 * t + 16 + 8 * g + 4 * h);
#pragma unroll
      for (int i = 0; i < 4; i += 2) {
        const f32x2 ag = {acc[t][4 * g + i], acc[t][4 * g + i + 1]}, au = {acc[t][8 + 4 * g + i], acc[t][8 + 4 * g + i + 1]};
        const f32x2 gv = __builtin_elementwise_fma(f32x2{cg[i], cg[i + 1]}, nm, ag);
        const f32x2 uv = __builtin_elementwise_fma(f32x2{cu[i], cu[i + 1]}, nm, au) * rr2;
        const f32x2 m = f32x2{fmaxf(gv[0], 0.f), fmaxf(gv[1], 0.f)} * uv;
        v[4 * g + i] = m[0]; v[4 * g + i + 1] = m[1];
      }
    }
    st16_dev(rws, lane * 16, dst + t * 2048, as_u4(pack8(v[0], v[1], v[2], v[3], v[4], v[5], v[6], v[7])));
  }
  WAIT_VM(0); BARRIER();
  signal_panel(ws, 2, p);
}

#define XD5 8
constexpr int P5_SLOT = 32768, P5_XD = XD5;
DI constexpr int p5_slot(int it) { return ((it + 3) & 3) * P5_SLOT; }

DI int p5_rot(int it, int rot) { const int v = it + rot; return v >= 7 ? v - 7 : v; }

DI void down_compute(char* smem, int w, int lane, char* ws, rsrc_t rws, char* out, int tp, int c, int rot) {
  const int r = lane & 31, h = lane >> 5, tt = w & 1, kh = w >> 1;
  const int tb = 2 * tp + tt, ntile = 2 * c + kh;
  u32x4 rh[2];
#pragma unroll
  for (int s = 0; s < 2; ++s) rh[s] = ld16_dev(rws, lane * 16, WS_H + (tb * 32 + 2 * ntile + s) * 1024);
  Polls pl;
  wait_panel(ws, 2, tp >> 1, (tp & 1) * 8 + c, w, pl);
  BARRIER();
  u32x4 xf[56];
  unsigned mb[7];
#pragma unroll
  for (int it = 0; it < 7; ++it) mb[it] = WS_M + ((tp * 112 + 56 * kh + 8 * p5_rot(it, rot)) * 2 + tt) * 1024;
  auto ldx = [&](int g) { xf[g] = ld16_dev(rws, lane * 16, mb[g >> 3] + (g & 7) * 2048); };
#pragma unroll
  for (int g = 0; g < P5_XD; ++g) ldx(g);
  const unsigned lb = lds_addr(smem) + kh * 16384;
  unsigned a0[2], a1[2];
#pragma unroll
  for (int b = 0; b < 2; ++b) { a0[b] = opaque(lb + wtr_off(lane, 0) + b * 65536); a1[b] = opaque(lb + wtr_off(lane, 1) + b * 65536); }
  bf16x8 wf[3][2];
  auto rd = [&](int g) {
    const int o = p5_slot(g >> 3) + (g & 7) * 2048, b = o >> 16, oo = o & 65535;
    wf[g % 3][0] = tr_frag_a(a0[b] + oo, a0[b] + oo + 1024);
    wf[g % 3][1] = tr_frag_a(a1[b] + oo, a1[b] + oo + 1024);
  };
  f32x16 acc[2] = {};
  rd(0); rd(1);
  SCHED_FENCE();
#pragma unroll
  for (int g = 0; g < 56; ++g) {
    const int n = g + 2;
    acc[0] = MFMA32(wf[g % 3][0], as_frag(xf[g]), acc[0]); SCHED_FENCE();
    if (g == 0) retire(pl);
    if (n < 56) {
      if ((n & 7) == 0) { WAIT_LGKM0(); BARRIER(); }
      rd(n);
    }
    if (g + P5_XD < 56) ldx(g + P5_XD);
    SCHED_FENCE();
    acc[1] = MFMA32(wf[g % 3][1], as_frag(xf[g]), acc[1]); SCHED_FENCE();
  }
  f32x16 a = pair_reduce<2>(smem, w, lane, acc);
  unsigned pk[8];
#pragma unroll
  for (int s = 0; s < 2; ++s) {
#pragma unroll
    for (int i = 0; i < 4; ++i) { a[8 * s + 2 * i] += bf_lo(rh[s][i]); a[8 * s + 2 * i + 1] += bf_hi(rh[s][i]); }
    const u32x4 u = as_u4(pack8(a[8 * s], a[8 * s + 1], a[8 * s + 2], a[8 * s + 3], a[8 * s + 4], a[8 * s + 5], a[8 * s + 6], a[8 * s + 7]));
#pragma unroll
    for (int i = 0; i < 4; ++i) pk[4 * s + i] = u[i];
  }
  char* dst = out + (size_t)(32 * tb + r) * 1024 + (32 * ntile + 8 * h) * 2;
#pragma unroll
  for (int s = 0; s < 2; ++s) {
    auto q0 = __builtin_amdgcn_permlane32_swap(pk[4 * s], pk[4 * s + 2], false, false);
    auto q1 = __builtin_amdgcn_permlane32_swap(pk[4 * s + 1], pk[4 * s + 3], false, false);
    *(u32x4*)(dst + s * 32) = u32x4{q0[0], q1[0], q0[1], q1[1]};
  }
}

DI void loader_345(char* smem, int lw, int lane, char* ws, rsrc_t rws, const char* wd, int j, int c, int rot) {
  f32x4 cv[8];
  const int cl = lane < 56 ? lane : 55;
#pragma unroll
  for (int i = 0; i < 8; ++i) cv[i] = *(const f32x4*)(ws + WS_CS + ((size_t)(8 * lw + i) * 112 + 7 * j) * 128 + cl * 16);
  SCHED_FENCE();
  auto wgu = [&](int s) {
#pragma unroll
    for (int i = 0; i < 7; ++i) blds16(rws, lane * 16, WS_WGU + (j * 32 * 7 + s * 28 + lw * 7 + i) * 1024, smem + (s % 5) * P4_SLOT + (lw * 7 + i) * 1024);
  };
  const rsrc_t rwd = make_rsrc(wd, F * D * 2);
  const unsigned vo = wrow_voff(lane);
  auto wdn = [&](int it) {
#pragma unroll
    for (int i = 0; i < 8; ++i) {
      const int pc = lw * 8 + i, kh = pc >> 4, rr = (pc & 15) * 8;
      blds16(rwd, vo, (896 * kh + 128 * p5_rot(it, rot) + rr) * 1024 + c * 128, smem + p5_slot(it) + pc * 1024);
    }
  };
  wgu(0); wgu(1);
  BARRIER();
  BARRIER();
  wgu(2); wgu(3); wgu(4);
  {
    f32x4 a = cv[0];
#pragma unroll
    for (int i = 1; i < 8; ++i) a += cv[i];
    if (lane < 56) *(f32x4*)(smem + L_CS + lw * 896 + lane * 16) = a;
  }
  WAIT_LGKM0(); BARRIER();
  if (lw == 0 && lane < 56) {
    const char* t = smem + L_CS + lane * 16;
    *(f32x4*)(smem + L_CS + lane * 16) = *(const f32x4*)t + *(const f32x4*)(t + 896) + *(const f32x4*)(t + 1792) + *(const f32x4*)(t + 2688);
  }
  WAIT_VM(28); WAIT_LGKM0(); BARRIER();
  WAIT_VM(21); BARRIER(); wgu(5);
  WAIT_VM(21); BARRIER(); wgu(6);
  WAIT_VM(21); BARRIER(); wgu(7);
  WAIT_VM(21); BARRIER();
  WAIT_VM(14); BARRIER(); wdn(0);
  WAIT_VM(15); BARRIER();
  WAIT_VM(8); BARRIER(); wdn(1);
  BARRIER(); wdn(2); wdn(3);
  BARRIER();
  WAIT_VM(24); BARRIER();
  WAIT_VM(16); BARRIER(); wdn(4);
  WAIT_VM(16); BARRIER(); wdn(5);
  WAIT_VM(16); BARRIER(); wdn(6);
  WAIT_VM(16); BARRIER();
  WAIT_VM(8); BARRIER();
  WAIT_VM(0); BARRIER();
  BARRIER();
}

extern "C" __global__ __launch_bounds__(512, 2) void k_main(char* __restrict__ ws, const char* __restrict__ x, const char* __restrict__ wo,
                                                 const char* __restrict__ wd, char* __restrict__ out) {
  extern __shared__ __attribute__((aligned(16))) char smem[];
  const int w = __builtin_amdgcn_readfirstlane(threadIdx.x >> 6), lane = threadIdx.x & 63;
  const rsrc_t rws = make_rsrc(ws, WS_BYTES);
  phase_attn(smem, w, lane, ws, rws, wo);
  const int xcd = blockIdx.x & 7, i = blockIdx.x >> 3;
  const int tp = 4 * xcd + (i & 3), c = i >> 2;
  const int p4 = 8 * (xcd & 1) + (i & 7), j4 = 4 * (xcd >> 1) + (i >> 3);
  const int rot = (c + 2 * (i & 3)) % 7;
  if (w >= 4) loader_345(smem, w - 4, lane, ws, rws, wd, j4, c, rot);
  else {
    oproj_compute(smem, w, lane, ws, rws, x, tp, c);
    gateup_compute(smem, w, lane, ws, rws, p4, j4);
    down_compute(smem, w, lane, ws, rws, out, tp, c, rot);
  }
}

extern "C" void kernel_launch(void* const* d_in, const int* in_sizes, int n_in, void* d_out, int out_size, void* d_ws, size_t ws_size,
                              hipStream_t stream) {
  const char* x = (const char*)d_in[0];
  const char* wqkv = (const char*)d_in[1];
  const char* wo = (const char*)d_in[2];
  const char* wg = (const char*)d_in[4];
  const char* wu = (const char*)d_in[5];
  const char* wd = (const char*)d_in[6];
  char* ws = (char*)d_ws;
  (void)hipFuncSetAttribute((const void*)k_qkv, hipFuncAttributeMaxDynamicSharedMemorySize, P1_LDS);
  (void)hipFuncSetAttribute((const void*)k_main, hipFuncAttributeMaxDynamicSharedMemorySize, MAIN_LDS);
  k_qkv<<<256, 512, P1_LDS, stream>>>(x, wqkv, wg, wu, ws, wo, wd, (const char*)d_out);
  k_main<<<256, 512, MAIN_LDS, stream>>>(ws, x, wo, wd, (char*)d_out);
}
```

```cpp
#include <hip/hip_runtime.h>
#include <hip/hip_bf16.h>
#include <stdint.h>

typedef __attribute__((ext_vector_type(8))) __bf16 bf16x8;
typedef __attribute__((ext_vector_type(2))) __bf16 bf16x2;
typedef __attribute__((ext_vector_type(16))) float f32x16;
typedef __attribute__((ext_vector_type(8))) float f32x8;
typedef __attribute__((ext_vector_type(4))) float f32x4;
typedef __attribute__((ext_vector_type(2))) float f32x2;
typedef __attribute__((ext_vector_type(4))) unsigned u32x4;
typedef __attribute__((ext_vector_type(2))) unsigned u32x2;
typedef __attribute__((ext_vector_type(4))) short s16x4;
typedef __attribute__((address_space(3))) s16x4* lds_s16x4;
typedef __attribute__((address_space(3))) void* lds_vp;
typedef const __attribute__((address_space(1))) void* g_vp;

#define POLL_GAP "4"
#define POLL_SLEEP ""
#define DI __device__ __forceinline__
#define MFMA32(a, b, c) __builtin_amdgcn_mfma_f32_32x32x16_bf16((a), (b), (c), 0, 0, 0)
#define BARRIER() asm volatile("s_barrier" ::: "memory")
#define WAIT_VM(n) asm volatile("s_waitcnt vmcnt(" #n ")" ::: "memory")
#define WAIT_LGKM0() asm volatile("s_waitcnt lgkmcnt(0)" ::: "memory")
#define SCHED_FENCE() __builtin_amdgcn_sched_barrier(0)

constexpr int S = 2048, D = 512, F = 1792;
constexpr float LN_EPS = 1e-5f;
constexpr float Q_SCALE = 0.25f * 1.4426950408889634f;

constexpr unsigned WS_Q = 0;
constexpr unsigned WS_K = WS_Q + (2u << 20);
constexpr unsigned WS_V = WS_K + (2u << 20);
constexpr unsigned WS_O = WS_V + (2u << 20);
constexpr unsigned WS_H = WS_O + (2u << 20);
constexpr unsigned WS_ST = WS_H + (2u << 20);
constexpr unsigned WS_M = WS_ST + (256u << 10);
constexpr unsigned WS_WGU = WS_M + (7u << 20);
constexpr unsigned WS_CS = WS_WGU + (3584u << 10);
constexpr unsigned WS_CNT = WS_CS + (448u << 10);
constexpr unsigned WS_BYTES = WS_CNT + 768 * 128;

typedef __amdgpu_buffer_rsrc_t rsrc_t;
typedef __attribute__((address_space(3))) const bf16x8* lds_fp;
typedef __attribute__((address_space(1))) unsigned gu32;
constexpr int SC1 = 16;
constexpr unsigned QKV_CODE_BYTES = 36864, MAIN_CODE_BYTES = 25216;

DI rsrc_t make_rsrc(const void* p, unsigned bytes) { return __builtin_amdgcn_make_buffer_rsrc((void*)p, 0, bytes, 0x00020000); }
DI void glds16(const void* g, void* l) { __builtin_amdgcn_global_load_lds((g_vp)g, (lds_vp)l, 16, 0, 0); }
DI void blds16(rsrc_t rs, unsigned voff, unsigned soff, char* l) { __builtin_amdgcn_raw_ptr_buffer_load_lds(rs, (lds_vp)l, 16, voff, soff, 0, 0); }
DI u32x4 ld16_dev(rsrc_t rs, unsigned voff, unsigned soff) { return __builtin_amdgcn_raw_buffer_load_b128(rs, voff, soff, SC1); }
DI void st16_dev(rsrc_t rs, unsigned voff, unsigned soff, u32x4 v) { __builtin_amdgcn_raw_buffer_store_b128(v, rs, voff + soff, 0, SC1); }
#define P1_AUXBITS 16
DI void st16_p1(rsrc_t rs, unsigned voff, unsigned soff, u32x4 v) { __builtin_amdgcn_raw_buffer_store_b128(v, rs, voff + soff, 0, P1_AUXBITS); }
DI bf16x8 lds_frag(const char* p) { return *(const bf16x8*)p; }
DI bf16x8 lds_frag_a(unsigned a) { return *(lds_fp)(size_t)a; }
DI unsigned lds_addr(const char* p) { return (unsigned)(size_t)(const __attribute__((address_space(3))) char*)p; }
DI unsigned touch(const char* g) { unsigned v; asm volatile("global_load_dword %0, %1, off" : "=v"(v) : "v"(g)); return v; }
#define KERNEL_ADDR(dst, sym) do { unsigned lo_, hi_; \
  asm volatile("s_getpc_b64 s[40:41]\n\ts_add_u32 s40, s40, " #sym "@rel32@lo+4\n\ts_addc_u32 s41, s41, " #sym "@rel32@hi+12\n\ts_mov_b32 %0, s40\n\ts_mov_b32 %1, s41" \
               : "=s"(lo_), "=s"(hi_) :: "s40", "s41", "scc"); \
  dst = ((unsigned long long)hi_ << 32) | lo_; } while (0)
DI void dma_piece(u32x4 rs, unsigned voff, unsigned soff, unsigned lds) {
  asm volatile("s_mov_b32 m0, %0\n\ts_nop 0\n\tbuffer_load_dwordx4 %1, %2, %3 offen lds" :: "s"(lds), "v"(voff), "s"(rs), "s"(soff));
}
DI unsigned opaque(unsigned v) { asm volatile("" : "+v"(v)); return v; }
DI bf16x8 tr_frag(const char* p0, const char* p1) {
  s16x4 lo = __builtin_amdgcn_ds_read_tr16_b64_v4i16((lds_s16x4)p0);
  s16x4 hi = __builtin_amdgcn_ds_read_tr16_b64_v4i16((lds_s16x4)p1);
  return __builtin_bit_cast(bf16x8, __builtin_shufflevector(lo, hi, 0, 1, 2, 3, 4, 5, 6, 7));
}
DI bf16x8 tr_frag_a(unsigned a0, unsigned a1) {
  s16x4 lo = __builtin_amdgcn_ds_read_tr16_b64_v4i16((lds_s16x4)(size_t)a0);
  s16x4 hi = __builtin_amdgcn_ds_read_tr16_b64_v4i16((lds_s16x4)(size_t)a1);
  return __builtin_bit_cast(bf16x8, __builtin_shufflevector(lo, hi, 0, 1, 2, 3, 4, 5, 6, 7));
}
DI bf16x8 pack8(float a0, float a1, float a2, float a3, float a4, float a5, float a6, float a7) {
  f32x8 v = {a0, a1, a2, a3, a4, a5, a6, a7};
  return __builtin_convertvector(v, bf16x8);
}
DI u32x4 as_u4(bf16x8 v) { return __builtin_bit_cast(u32x4, v); }
DI bf16x8 as_frag(u32x4 v) { return __builtin_bit_cast(bf16x8, v); }
DI float bf_lo(unsigned u) { return __uint_as_float(u << 16); }
DI float bf_hi(unsigned u) { return __uint_as_float(u & 0xffff0000u); }
DI float half_swap_sum(float v) {
  auto r = __builtin_amdgcn_permlane32_swap(__float_as_uint(v), __float_as_uint(v), false, false);
  return __uint_as_float(r[0]) + __uint_as_float(r[1]);
}
DI float half_swap_max(float v) {
  auto r = __builtin_amdgcn_permlane32_swap(__float_as_uint(v), __float_as_uint(v), false, false);
  return fmaxf(__uint_as_float(r[0]), __uint_as_float(r[1]));
}
DI void add_f32(float& a, float v) { asm("v_add_f32 %0, %0, %1" : "+v"(a) : "v"(v)); }
DI void fma_sq(float& a, float v) { asm("v_fma_f32 %0, %1, %1, %0" : "+v"(a) : "v"(v)); }
DI float colsum_frag(bf16x8 f, float acc) {
  const bf16x2 ones = {(__bf16)1.0f, (__bf16)1.0f};
  const u32x4 u = as_u4(f);
#pragma unroll
  for (int i = 0; i < 4; ++i) { const unsigned e = u[i]; acc = __builtin_amdgcn_fdot2_f32_bf16(__builtin_bit_cast(bf16x2, e), ones, acc, false); }
  return acc;
}

DI void signal_panel(char* ws, int phase, int panel) {
  if (threadIdx.x < 16)
    __hip_atomic_fetch_add((gu32*)(ws + WS_CNT + ((phase * 16 + panel) * 16 + threadIdx.x) * 128), 1u, __ATOMIC_RELAXED, __HIP_MEMORY_SCOPE_AGENT);
}
struct Polls { unsigned v[4]; };
DI void wait_panel(char* ws, int phase, int panel, int replica, int w, Polls& p) {
  const char* word = ws + WS_CNT + ((phase * 16 + panel) * 16 + replica) * 128;
  unsigned left = 1u << 17, seen;
  unsigned zero = 0;
  asm volatile(
      "s_cmp_lg_u32 %8, 0\n\ts_cbranch_scc1 2f\n"
      "1:\n\tglobal_load_dword %0, %6, %7 sc1\n\ts_waitcnt vmcnt(0)\n\tv_readfirstlane_b32 %5, %0\n\ts_cmp_ge_u32 %5, 16\n\ts_cbranch_scc1 2f\n\t" POLL_SLEEP
      "s_sub_u32 %4, %4, 1\n\ts_cmp_lg_u32 %4, 0\n\ts_cbranch_scc1 1b\n"
      "2:"
      : "=&v"(p.v[0]), "=&v"(p.v[1]), "=&v"(p.v[2]), "=&v"(p.v[3]), "+s"(left), "=&s"(seen)
      : "v"(zero), "s"(word), "s"(w)
      : "scc", "memory");
  return;
#define POLL_STEP(k) "s_waitcnt vmcnt(3)\n\tv_readfirstlane_b32 %5, %" #k "\n\ts_cmp_ge_u32 %5, 16\n\ts_cbranch_scc1 2f\n\tglobal_load_dword %" #k ", %6, %7 sc1\n\t"
  asm volatile(
      "s_cmp_lg_u32 %8, 0\n\ts_cbranch_scc1 2f\n\t"
      "global_load_dword %0, %6, %7 sc1\n\ts_sleep " POLL_GAP "\n\t"
      "global_load_dword %1, %6, %7 sc1\n\ts_sleep " POLL_GAP "\n\t"
      "global_load_dword %2, %6, %7 sc1\n\ts_sleep " POLL_GAP "\n\t"
      "global_load_dword %3, %6, %7 sc1\n"
      "1:\n\t" POLL_STEP(0) POLL_STEP(1) POLL_STEP(2) POLL_STEP(3)
      "s_sub_u32 %4, %4, 1\n\ts_cmp_lg_u32 %4, 0\n\ts_cbranch_scc1 1b\n"
      "2:"
      : "=&v"(p.v[0]), "=&v"(p.v[1]), "=&v"(p.v[2]), "=&v"(p.v[3]), "+s"(left), "=&s"(seen)
      : "v"(zero), "s"(word), "s"(w)
      : "scc", "memory");
#undef POLL_STEP
}
DI void retire(const Polls& p) { asm volatile("; retire %0 %1 %2 %3" :: "v"(p.v[0]), "v"(p.v[1]), "v"(p.v[2]), "v"(p.v[3])); }
constexpr int P1_SLOT = 28672, P1_NSLOT = 5, P1_AUX = P1_SLOT * P1_NSLOT, P1_PREP = P1_AUX + 4096;
constexpr int P1_LDS = P1_PREP + 16384;

template <int VMASK>
DI void p1_compute(char* smem, int w, int lane, int p, int j, const char* wqkv, char* ws) {
  const int r = lane & 31, h = lane >> 5;
  const int row = 32 * w + r, sw = (r >> 1) & 7;
  const unsigned lb = lds_addr(smem);
  unsigned xa[4][3], wa[3];
#pragma unroll
  for (int ks = 0; ks < 4; ++ks) {
    const unsigned o = lb + row * 128 + (((2 * ks + h) ^ sw) << 4);
#pragma unroll
    for (int b = 0; b < 3; ++b) xa[ks][b] = opaque(o + b * 2 * P1_SLOT);
  }
  {
    const int q = (lane & 15) >> 2, pp = lane & 3, blk = (lane >> 4) & 1;
    const unsigned o = lb + 16384 + (8 * h + q) * 192 + (2 * blk + (pp >> 1)) * 16 + 8 * (pp & 1);
#pragma unroll
    for (int b = 0; b < 3; ++b) wa[b] = opaque(o + b * 2 * P1_SLOT);
  }
  f32x16 acc[3] = {};
  float cs[3] = {0.f, 0.f, 0.f};
  bf16x8 xf[3], wf[3][3];
  auto rdx = [&](int g) { const int sl = (g >> 2) % P1_NSLOT; xf[g % 3] = lds_frag_a(xa[g & 3][sl >> 1] + (sl & 1) * P1_SLOT); };
  auto rdw = [&](int g, int c) {
    const int sl = (g >> 2) % P1_NSLOT;
    const unsigned a = wa[sl >> 1] + (sl & 1) * P1_SLOT + (16 * (g & 3)) * 192 + c * 64;
    wf[g % 3][c] = tr_frag_a(a, a + 4 * 192);
  };
  auto mm = [&](int g, int c) {
    if ((VMASK >> c) & 1) acc[c] = MFMA32(xf[g % 3], wf[g % 3][c], acc[c]);
    else acc[c] = MFMA32(wf[g % 3][c], xf[g % 3], acc[c]);
  };
  const unsigned long long wa64 = (unsigned long long)wqkv;
  const u32x4 rw = {(unsigned)wa64, (unsigned)(wa64 >> 32) & 0xffffu, (unsigned)(D * 3 * D * 2), 0x00020000u};
  unsigned wo_[3];
#pragma unroll
  for (int i = 0; i < 3; ++i) { const int g = 64 * (3 * w + i) + lane; wo_[i] = (g / 12) * 3072 + 192 * j + (g % 12) * 16; }
  auto wpiece = [&](int s, int i) { dma_piece(rw, wo_[i], s * 64 * 3072, lb + (s % P1_NSLOT) * P1_SLOT + 16384 + (3 * w + i) * 1024); };
#pragma unroll
  for (int s = 0; s < 3; ++s) { wpiece(s, 0); wpiece(s, 1); wpiece(s, 2); }
  unsigned warm;
  {
    unsigned long long ka, kb;
    KERNEL_ADDR(ka, k_qkv); KERNEL_ADDR(kb, k_main);
    const unsigned i = blockIdx.x >> 3, v = 4 * w + (lane & 3);
    const char* src = v < 9 ? (const char*)ka + min((i * 9 + v) * 128u, QKV_CODE_BYTES - 128u) : (const char*)kb + min((i * 7 + v - 9) * 128u, MAIN_CODE_BYTES - 128u);
    asm volatile("global_load_dword %0, %1, off" : "=v"(warm) : "v"(src));
  }
  WAIT_VM(7); BARRIER();
#pragma unroll
  for (int g = 0; g < 2; ++g) { rdx(g); rdw(g, 0); rdw(g, 1); rdw(g, 2); }
  SCHED_FENCE();
#pragma unroll
  for (int g = 0; g < 32; ++g) {
    const int n = g + 2;
    mm(g, 0); SCHED_FENCE();
    if (n < 32) {
      if ((n & 3) == 0) {
        if (n <= 8) WAIT_VM(7); else if (n <= 20) WAIT_VM(6); else if (n == 24) WAIT_VM(3); else WAIT_VM(0);
        WAIT_LGKM0(); BARRIER();
      }
      rdx(n); rdw(n, 0); SCHED_FENCE();
    }
    mm(g, 1); SCHED_FENCE();
    {
      const int m = g < 2 ? g : g + 2;
      const int s = g < 2 ? 3 : (m >> 2) + 3, i = g < 2 ? g : (m & 3);
      if (s < 8 && i < 3) { wpiece(s, i); SCHED_FENCE(); }
      if (g == 1) { wpiece(3, 2); SCHED_FENCE(); }
    }
    if (n < 32) { rdw(n, 1); rdw(n, 2); SCHED_FENCE(); }
    mm(g, 2); SCHED_FENCE();
    if ((g & 3) == w) {
#pragma unroll
      for (int c = 0; c < 3; ++c) cs[c] = colsum_frag(wf[g % 3][c], cs[c]);
    }
    SCHED_FENCE();
  }
  asm volatile("; warm %0" :: "v"(warm));
  float* csp = (float*)(smem + P1_AUX);
  float* cst = csp + 384;
  f32x2* st = (f32x2*)(smem + P1_AUX + 2048);
#pragma unroll
  for (int c = 0; c < 3; ++c) { float t = half_swap_sum(cs[c]); if (h == 0) csp[w * 96 + c * 32 + r] = t; }
  WAIT_LGKM0(); BARRIER();
  if (w < 2) { int t = w * 64 + lane; if (t < 96) cst[t] = csp[t] + csp[96 + t] + csp[192 + t] + csp[288 + t]; }
  WAIT_LGKM0(); BARRIER();
  const int tb = 4 * p + w;
  const rsrc_t rws = make_rsrc(ws, WS_BYTES);
  const f32x2 mine = st[row];
#pragma unroll
  for (int c = 0; c < 3; ++c) {
    const int c0 = 96 * j + 32 * c;
    if ((VMASK >> c) & 1) {
      const float csv = cst[32 * c + r];
      float v[16];
#pragma unroll
      for (int i = 0; i < 16; ++i) { f32x2 t = st[32 * w + (i & 3) + 8 * (i >> 2) + 4 * h]; v[i] = acc[c][i] * t[0] - t[1] * csv; }
      const unsigned dst = WS_V + (((c0 - 1024) >> 5) * 128 + 2 * tb) * 1024;
      st16_p1(rws, lane * 16, dst, as_u4(pack8(v[0], v[1], v[2], v[3], v[4], v[5], v[6], v[7])));
      st16_p1(rws, lane * 16, dst + 1024, as_u4(pack8(v[8], v[9], v[10], v[11], v[12], v[13], v[14], v[15])));
    } else {
      const bool isq = c0 < 512;
      const float sc = isq ? Q_SCALE : 1.0f;
      const float a = mine[0] * sc, b = mine[1] * sc;
      float v[16];
#pragma unroll
      for (int g = 0; g < 4; ++g) {
        f32x4 cv = *(const f32x4*)(cst + 32 * c + 8 * g + 4 * h);
#pragma unroll
        for (int i = 0; i < 4; ++i) v[4 * g + i] = acc[c][4 * g + i] * a - b * cv[i];
      }
      const int head = (c0 & 511) >> 4;
      const unsigned dst = (isq ? WS_Q : WS_K) + (head * 64 + tb) * 1024;
      st16_p1(rws, lane * 16, dst, as_u4(pack8(v[0], v[1], v[2], v[3], v[4], v[5], v[6], v[7])));
      st16_p1(rws, lane * 16, dst + 65536, as_u4(pack8(v[8], v[9], v[10], v[11], v[12], v[13], v[14], v[15])));
    }
  }
}

DI void p1_helper(char* smem, int lw, int lane, int p, int j, const char* x, const char* wg, const char* wu, char* ws) {
  const rsrc_t rx = make_rsrc(x, S * D * 2);
  unsigned xo[4];
#pragma unroll
  for (int i = 0; i < 4; ++i) {
    int row = 32 * lw + 8 * i + (lane >> 3);
    xo[i] = (128 * p + row) * 1024 + (((lane & 7) ^ ((row >> 1) & 7)) << 4);
  }
  auto issue = [&](int s) {
    char* sl = smem + (s % P1_NSLOT) * P1_SLOT;
#pragma unroll
    for (int i = 0; i < 4; ++i) blds16(rx, xo[i], s * 128, sl + (4 * lw + i) * 1024);
  };
  issue(0); issue(1); issue(2);
  const int b = blockIdx.x, cg = b % 28, pks = 4 * (b / 28) + lw;
  const bool prep = b < 224;
  char* stg = smem + P1_PREP + lw * 4096;
  auto prep_issue = [&]() {
    const rsrc_t rg = make_rsrc(wg, D * F * 2), ru = make_rsrc(wu, D * F * 2);
    const unsigned off = (16 * pks + (lane >> 3)) * 3584 + cg * 128 + (lane & 7) * 16;
    blds16(rg, off, 0, stg); blds16(rg, off, 8 * 3584, stg + 1024);
    blds16(ru, off, 0, stg + 2048); blds16(ru, off, 8 * 3584, stg + 3072);
  };
  const int r = lane & 31, h = lane >> 5, row = 32 * lw + r, sw = (r >> 1) & 7;
  bf16x8 pf_[4];
  auto prep_read = [&]() {
    const int blk = (lane >> 4) & 1, q = (lane & 15) >> 2, pp = lane & 3;
    const unsigned bs = lds_addr(stg) + blk * 2048 + (4 * h + q) * 128 + (pp >> 1) * 16 + 8 * (pp & 1);
    u32x2 t[8];
#define PREP_RD(i, off) asm volatile("ds_read_b64_tr_b16 %0, %1 offset:" #off : "=v"(t[i]) : "v"(bs))
    PREP_RD(0, 0); PREP_RD(1, 1024); PREP_RD(2, 32); PREP_RD(3, 1056); PREP_RD(4, 64); PREP_RD(5, 1088); PREP_RD(6, 96); PREP_RD(7, 1120);
#undef PREP_RD
    asm volatile("s_waitcnt lgkmcnt(0)" : "+v"(t[0]), "+v"(t[1]), "+v"(t[2]), "+v"(t[3]), "+v"(t[4]), "+v"(t[5]), "+v"(t[6]), "+v"(t[7]));
#pragma unroll
    for (int i = 0; i < 4; ++i) { const u32x2 lo = t[2 * i], hi = t[2 * i + 1]; pf_[i] = as_frag(u32x4{lo[0], lo[1], hi[0], hi[1]}); }
  };
  auto prep_frag = [&](int i) {
    const int fs = 4 * cg + i, jj = fs / 7, t = fs % 7;
    st16_p1(make_rsrc(ws, WS_BYTES), lane * 16, WS_WGU + ((jj * 32 + pks) * 7 + t) * 1024, as_u4(pf_[i]));
    const float c = half_swap_sum(colsum_frag(pf_[i], 0.f));
    if (h == 0) *(float*)(ws + WS_CS + ((size_t)pks * 112 + fs) * 128 + r * 4) = c;
  };
  float sum0 = 0.f, sum1 = 0.f, sq0 = 0.f, sq1 = 0.f;
  auto piece = [&](int s, int i) { blds16(rx, xo[i], s * 128, smem + (s % P1_NSLOT) * P1_SLOT + (4 * lw + i) * 1024); };
  auto step = [&](int s, int n) {
    const char* sl = smem + (s % P1_NSLOT) * P1_SLOT + row * 128;
    u32x4 u[4];
#pragma unroll
    for (int c = 0; c < 4; ++c) u[c] = *(const u32x4*)(sl + (((4 * h + c) ^ sw) << 4));
    SCHED_FENCE();
#pragma unroll
    for (int c = 0; c < 4; ++c) {
      if (n < 8) piece(n, c);
      SCHED_FENCE();
#pragma unroll
      for (int i = 0; i < 4; ++i) {
        const float lo = bf_lo(u[c][i]), hi = bf_hi(u[c][i]);
        add_f32(sum0, lo); add_f32(sum1, hi); fma_sq(sq0, lo); fma_sq(sq1, hi);
      }
      SCHED_FENCE();
    }
  };
  if (prep) {
    prep_issue();
    WAIT_VM(12); BARRIER();
    step(0, 3); WAIT_VM(12); WAIT_LGKM0(); BARRIER();
    step(1, 4); WAIT_VM(12); WAIT_LGKM0(); BARRIER();
  } else {
    WAIT_VM(8); BARRIER();
    step(0, 3); WAIT_VM(8); WAIT_LGKM0(); BARRIER();
    step(1, 4); WAIT_VM(8); WAIT_LGKM0(); BARRIER();
  }
  step(2, 5); WAIT_VM(8); WAIT_LGKM0(); BARRIER();
  step(3, 6); WAIT_VM(8); WAIT_LGKM0(); BARRIER();
  step(4, 7); WAIT_VM(8); WAIT_LGKM0(); BARRIER();
  step(5, 8); WAIT_VM(4);
  if (prep) prep_read();
  WAIT_LGKM0(); BARRIER();
  step(6, 8); WAIT_VM(0);
  if (prep) prep_frag(0);
  WAIT_LGKM0(); BARRIER();
  step(7, 8);
  const float s1 = half_swap_sum(sum0 + sum1), s2 = half_swap_sum(sq0 + sq1);
  const float mean = s1 * (1.0f / D);
  const float var = fmaxf(s2 * (1.0f / D) - mean * mean, 0.f);
  const float rstd = 1.0f / sqrtf(var + LN_EPS);
  f32x2* st = (f32x2*)(smem + P1_AUX + 2048);
  if (h == 0) st[row] = f32x2{rstd, mean * rstd};
  WAIT_LGKM0(); BARRIER();
  BARRIER();
  if (prep) prep_frag(1);
  if (prep) { prep_frag(2); prep_frag(3); }
}

extern "C" __global__ __launch_bounds__(512, 2) void k_qkv(const char* __restrict__ x, const char* __restrict__ wqkv, const char* __restrict__ wg,
                                                const char* __restrict__ wu, char* __restrict__ ws) {
  extern __shared__ __attribute__((aligned(16))) char smem[];
  const int w = __builtin_amdgcn_readfirstlane(threadIdx.x >> 6), lane = threadIdx.x & 63;
  const int xcd = blockIdx.x & 7, i = blockIdx.x >> 3;
  const int p = 4 * (xcd & 3) + (i & 3), j = 8 * (xcd >> 2) + (i >> 2);
  if (threadIdx.x < 3) *(unsigned*)(ws + WS_CNT + (blockIdx.x * 3 + threadIdx.x) * 128) = 0u;
  if (w >= 4) p1_helper(smem, w - 4, lane, p, j, x, wg, wu, ws);
  else if (j <= 9) p1_compute<0>(smem, w, lane, p, j, wqkv, ws);
  else if (j == 10) p1_compute<4>(smem, w, lane, p, j, wqkv, ws);
  else p1_compute<7>(smem, w, lane, p, j, wqkv, ws);
}

constexpr int P2_K = 0, P2_V = 24576, P2_VP = 1040, P2_Q = P2_V + 24 * P2_VP;
constexpr int L_WO = 77824;
constexpr int L_RED = 143360;
constexpr int L_CS = L_RED + 16384;
constexpr int MAIN_LDS = 163840;

template <bool FULL>
DI void attn_task(const char* smem, int lane, int hh, int wq, int qb, rsrc_t rws, int head) {
  const int r = lane & 31, h = lane >> 5;
  const char* kb_ = smem + P2_K + (hh * 12 + wq) * 1024 + lane * 16;
  const bool valid = (r >> 4) == hh;
  const char* vb_ = smem + P2_V + 2 * wq * P2_VP + (valid ? lane * 16 : 1024);
  const bf16x8 qf = lds_frag(smem + P2_Q + (hh * 4 + wq) * 1024 + lane * 16);
  const f32x16 zero = {};
  const int t0 = FULL ? 1 : ((8 - qb) > 1 ? (8 - qb) : 1);
  const bool have_first = FULL || qb >= 8;
  f32x16 sm;
  {
    const f32x16 sd = MFMA32(lds_frag(kb_ + 8 * 1024), qf, zero);
    f32x16 sf;
    if (have_first) sf = MFMA32(lds_frag(kb_), qf, zero);
    else { for (int i = 0; i < 16; ++i) sf[i] = -1e30f; }
#pragma unroll
    for (int i = 0; i < 16; ++i) { const int jj = (i & 3) + 8 * (i >> 2) + 4 * h; sm[i] = jj <= r ? sd[i] : sf[i]; }
  }
  float m = sm[0];
#pragma unroll
  for (int i = 1; i < 16; ++i) m = fmaxf(m, sm[i]);
  if (FULL) {
#pragma unroll
    for (int t = 1; t < 8; ++t) {
      const f32x16 s = MFMA32(lds_frag(kb_ + t * 1024), qf, zero);
#pragma unroll
      for (int i = 0; i < 16; i += 2) m = fmaxf(fmaxf(m, s[i]), s[i + 1]);
    }
  } else {
    for (int t = t0; t < 8; ++t) {
      const f32x16 s = MFMA32(lds_frag(kb_ + t * 1024), qf, zero);
#pragma unroll
      for (int i = 0; i < 16; i += 2) m = fmaxf(fmaxf(m, s[i]), s[i + 1]);
    }
  }
  m = half_swap_max(m);
  f32x16 negm;
#pragma unroll
  for (int i = 0; i < 16; ++i) negm[i] = -m;
  f32x16 o = zero;
  {
    float pv[16];
#pragma unroll
    for (int i = 0; i < 16; ++i) pv[i] = __builtin_amdgcn_exp2f(sm[i] - m);
    u32x4 pa = __builtin_bit_cast(u32x4, pack8(pv[0], pv[1], pv[2], pv[3], pv[4], pv[5], pv[6], pv[7]));
    u32x4 pb = __builtin_bit_cast(u32x4, pack8(pv[8], pv[9], pv[10], pv[11], pv[12], pv[13], pv[14], pv[15]));
    u32x4 da, db;
#pragma unroll
    for (int i = 0; i < 4; ++i) {
      const int ja = ((2 * i) & 3) + 8 * ((2 * i) >> 2) + 4 * h;
      const unsigned ma = (ja <= r ? 0xffffu : 0u) | (ja + 1 <= r ? 0xffff0000u : 0u);
      const unsigned mb = (ja + 16 <= r ? 0xffffu : 0u) | (ja + 17 <= r ? 0xffff0000u : 0u);
      da[i] = pa[i] & ma; db[i] = pb[i] & mb;
    }
    o = MFMA32(lds_frag(vb_ + 16 * P2_VP), __builtin_bit_cast(bf16x8, da), o);
    o = MFMA32(lds_frag(vb_ + 17 * P2_VP), __builtin_bit_cast(bf16x8, db), o);
    if (have_first) {
      o = MFMA32(lds_frag(vb_), __builtin_bit_cast(bf16x8, pa ^ da), o);
      o = MFMA32(lds_frag(vb_ + P2_VP), __builtin_bit_cast(bf16x8, pb ^ db), o);
    }
  }
  auto tile = [&](int t) {
    const f32x16 s = MFMA32(lds_frag(kb_ + t * 1024), qf, negm);
    float pv[16];
#pragma unroll
    for (int i = 0; i < 16; ++i) pv[i] = __builtin_amdgcn_exp2f(s[i]);
    o = MFMA32(lds_frag(vb_ + (2 * t) * P2_VP), pack8(pv[0], pv[1], pv[2], pv[3], pv[4], pv[5], pv[6], pv[7]), o);
    o = MFMA32(lds_frag(vb_ + (2 * t + 1) * P2_VP), pack8(pv[8], pv[9], pv[10], pv[11], pv[12], pv[13], pv[14], pv[15]), o);
  };
  if (FULL) {
#pragma unroll
    for (int t = 1; t < 8; ++t) tile(t);
  } else {
    for (int t = t0; t < 8; ++t) tile(t);
  }
  const float l = hh ? o[0] : o[8];
  const float inv = 1.0f / l;
  float ov[8];
#pragma unroll
  for (int i = 0; i < 8; ++i) ov[i] = (hh ? o[8 + i] : o[i]) * inv;
  st16_dev(rws, lane * 16, WS_O + (((qb >> 1) * 32 + head) * 2 + (qb & 1)) * 1024, as_u4(pack8(ov[0], ov[1], ov[2], ov[3], ov[4], ov[5], ov[6], ov[7])));
}

DI unsigned wrow_voff(int lane) { return (lane >> 3) * 1024 + (((lane & 7) ^ (((lane >> 4) & 1) << 2)) << 4); }
DI unsigned wtr_off(int lane, int nt) {
  const int h = lane >> 5, blk = (lane >> 4) & 1, q = (lane & 15) >> 2, pp = lane & 3, sw = (q >> 1) & 1;
  return (4 * h + q) * 128 + (2 * blk + (pp >> 1) + 4 * (nt ^ sw)) * 16 + 8 * (pp & 1);
}

DI void phase_attn(char* smem, int w, int lane, char* ws, rsrc_t rws, const char* wo) {
  const int xcd = blockIdx.x & 7, i = blockIdx.x >> 3;
  const int hp = 2 * xcd + (i & 1), p = i >> 1;
  const int kb0 = 4 * p - 8;
  const int hh = w >> 2, wq = w & 3, qb = 4 * p + wq, head = 2 * hp + hh;
  blds16(rws, lane * 16, WS_Q + (head * 64 + qb) * 1024, smem + P2_Q + w * 1024);
#pragma unroll
  for (int n = 0; n < 3; ++n) {
    const int t = wq * 3 + n, sv = w * 3 + n;
    const int kt = kb0 + t < 0 ? 0 : kb0 + t, vt = 2 * kb0 + sv < 0 ? 0 : 2 * kb0 + sv;
    blds16(rws, lane * 16, WS_K + (head * 64 + kt) * 1024, smem + P2_K + (hh * 12 + t) * 1024);
    blds16(rws, lane * 16, WS_V + (hp * 128 + vt) * 1024, smem + P2_V + sv * P2_VP);
  }
  if (threadIdx.x < 24) *(u32x4*)(smem + P2_V + threadIdx.x * P2_VP + 1024) = u32x4{0x3f803f80u, 0x3f803f80u, 0x3f803f80u, 0x3f803f80u};
  WAIT_VM(0); WAIT_LGKM0(); BARRIER();
  {
    const rsrc_t rwo = make_rsrc(wo, D * D * 2);
    const int c = i >> 2;
    const unsigned vo = wrow_voff(lane);
#pragma unroll
    for (int n = 0; n < 8; ++n) blds16(rwo, vo, (8 * w + n) * 8192 + c * 128, smem + L_WO + (8 * w + n) * 1024);
  }
  if (qb >= 8) attn_task<true>(smem, lane, hh, wq, qb, rws, head);
  else attn_task<false>(smem, lane, hh, wq, qb, rws, head);
  WAIT_VM(0); WAIT_LGKM0(); BARRIER();
  signal_panel(ws, 0, p);
}

template <int NACC>
DI f32x16 pair_reduce(char* smem, int w, int lane, const f32x16 (&acc)[NACC]) {
  char* dst = smem + L_RED + w * 4096 + lane * 16;
  auto put = [&](const f32x16& send) {
#pragma unroll
    for (int g = 0; g < 4; ++g) *(f32x4*)(dst + g * 1024) = f32x4{send[4 * g], send[4 * g + 1], send[4 * g + 2], send[4 * g + 3]};
  };
  f32x16 a;
  if (w >> 1) { put(acc[0]); a = acc[1]; asm volatile("" : "+v"(a)); }
  else { put(acc[1]); a = acc[0]; asm volatile("" : "+v"(a)); }
  WAIT_LGKM0(); BARRIER();
#pragma unroll
  for (int g = 0; g < 4; ++g) {
    const f32x4 v = *(const f32x4*)(smem + L_RED + ((w ^ 2) * 4 + g) * 1024 + lane * 16);
    a[4 * g] += v[0]; a[4 * g + 1] += v[1]; a[4 * g + 2] += v[2]; a[4 * g + 3] += v[3];
  }
  return a;
}

DI void oproj_compute(char* smem, int w, int lane, char* ws, rsrc_t rws, const char* x, int tp, int c) {
  const int r = lane & 31, h = lane >> 5, tt = w & 1, kh = w >> 1;
  const int tb = 2 * tp + tt, ntile = 2 * c + kh;
  u32x2 rx[4];
#pragma unroll
  for (int g = 0; g < 4; ++g) rx[g] = *(const u32x2*)(x + (size_t)(32 * tb + r) * 1024 + (32 * ntile + 8 * g + 4 * h) * 2);
  Polls pl;
  wait_panel(ws, 0, tp >> 1, (tp & 1) * 8 + c, w, pl);
  BARRIER();
  u32x4 xf[16];
#pragma unroll
  for (int g = 0; g < 16; ++g) xf[g] = ld16_dev(rws, lane * 16, WS_O + ((tp * 32 + 16 * kh + g) * 2 + tt) * 1024);
  const unsigned lb = lds_addr(smem) + L_WO + kh * 32768;
  const unsigned a0 = opaque(lb + wtr_off(lane, 0)), a1 = opaque(lb + wtr_off(lane, 1));
  bf16x8 wf[3][2];
  auto rd = [&](int g) {
    wf[g % 3][0] = tr_frag_a(a0 + g * 2048, a0 + g * 2048 + 1024);
    wf[g % 3][1] = tr_frag_a(a1 + g * 2048, a1 + g * 2048 + 1024);
  };
  f32x16 acc[2] = {};
  rd(0); rd(1);
  SCHED_FENCE();
#pragma unroll
  for (int g = 0; g < 16; ++g) {
    acc[0] = MFMA32(wf[g % 3][0], as_frag(xf[g]), acc[0]); SCHED_FENCE();
    if (g == 0) retire(pl);
    if (g + 2 < 16) { rd(g + 2); SCHED_FENCE(); }
    acc[1] = MFMA32(wf[g % 3][1], as_frag(xf[g]), acc[1]); SCHED_FENCE();
  }
  f32x16 a = pair_reduce<2>(smem, w, lane, acc);
#pragma unroll
  for (int g = 0; g < 4; ++g) {
    a[4 * g] += bf_lo(rx[g][0]); a[4 * g + 1] += bf_hi(rx[g][0]); a[4 * g + 2] += bf_lo(rx[g][1]); a[4 * g + 3] += bf_hi(rx[g][1]);
  }
  float s1 = 0.f, s2 = 0.f;
#pragma unroll
  for (int s = 0; s < 2; ++s) {
    const u32x4 u = as_u4(pack8(a[8 * s], a[8 * s + 1], a[8 * s + 2], a[8 * s + 3], a[8 * s + 4], a[8 * s + 5], a[8 * s + 6], a[8 * s + 7]));
    st16_dev(rws, lane * 16, WS_H + (tb * 32 + 2 * ntile + s) * 1024, u);
#pragma unroll
    for (int i = 0; i < 4; ++i) { const float lo = bf_lo(u[i]), hi = bf_hi(u[i]); s1 += lo + hi; s2 += lo * lo + hi * hi; }
  }
  s1 = half_swap_sum(s1); s2 = half_swap_sum(s2);
  if (h == 0) __builtin_amdgcn_raw_buffer_store_b64(u32x2{__float_as_uint(s1), __float_as_uint(s2)}, rws, r * 8, WS_ST + (tb * 16 + ntile) * 256, SC1);
  WAIT_VM(0); BARRIER();
  signal_panel(ws, 1, tp >> 1);
}

#define XD4 12
constexpr int P4_SLOT = 28672, P4_XD = XD4;

DI void gateup_compute(char* smem, int w, int lane, char* ws, rsrc_t rws, int p, int j) {
  const int r = lane & 31, h = lane >> 5, tb = 4 * p + w;
  Polls pl;
  wait_panel(ws, 1, p, j, w, pl);
  BARRIER();
  u32x4 xf[32];
  auto ldx = [&](int g) { xf[g] = ld16_dev(rws, lane * 16, WS_H + (tb * 32 + g) * 1024); };
  ldx(0); ldx(1);
  u32x2 sv[8];
#pragma unroll
  for (int i = 0; i < 8; ++i) sv[i] = __builtin_amdgcn_raw_buffer_load_b64(rws, (8 * h + i) * 256 + r * 8, WS_ST + tb * 4096, SC1);
#pragma unroll
  for (int g = 2; g < P4_XD; ++g) ldx(g);
  const unsigned lb = lds_addr(smem) + lane * 16;
  unsigned ba[3];
#pragma unroll
  for (int b = 0; b < 3; ++b) ba[b] = opaque(lb + b * 2 * P4_SLOT);
  bf16x8 wf[2][7];
  auto rdw = [&](int g, int t) {
    const int sl = (g >> 2) % 5;
    wf[g & 1][t] = lds_frag_a(ba[sl >> 1] + (sl & 1) * P4_SLOT + ((g & 3) * 7 + t) * 1024);
  };
  f32x16 acc[7] = {};
#pragma unroll
  for (int t = 0; t < 7; ++t) rdw(0, t);
  float s1 = 0.f, s2 = 0.f;
  SCHED_FENCE();
#pragma unroll
  for (int g = 0; g < 32; ++g) {
    const int n = g + 1;
#pragma unroll
    for (int t = 0; t < 7; ++t) {
      acc[t] = MFMA32(wf[g & 1][t], as_frag(xf[g]), acc[t]); SCHED_FENCE();
      if (g == 0 && t == 0) retire(pl);
      if (t == 0 && n < 32 && (n & 3) == 0) { WAIT_LGKM0(); BARRIER(); }
      if (t == 0 && n == 32) { WAIT_LGKM0(); BARRIER(); }
      if (n < 32) rdw(n, t);
      if (t == 1 && g + P4_XD < 32) ldx(g + P4_XD);
      if (g == 2 && t == 2) {
#pragma unroll
        for (int i = 0; i < 8; ++i) { const unsigned e0 = sv[i][0], e1 = sv[i][1]; s1 += __uint_as_float(e0); s2 += __uint_as_float(e1); }
      }
      SCHED_FENCE();
    }
  }
  s1 = half_swap_sum(s1); s2 = half_swap_sum(s2);
  const float mean = s1 * (1.0f / D);
  const float rstd = 1.0f / sqrtf(fmaxf(s2 * (1.0f / D) - mean * mean, 0.f) + LN_EPS);
  const float rr = rstd * rstd;
  const f32x2 nm = {-mean, -mean}, rr2 = {rr, rr};
  const float* cst = (const float*)(smem + L_CS);
  const unsigned dst = WS_M + (((tb >> 1) * 112 + 7 * j) * 2 + (tb & 1)) * 1024;
#pragma unroll
  for (int t = 0; t < 7; ++t) {
    float v[8];
#pragma unroll
    for (int g = 0; g < 2; ++g) {
      const f32x4 cg = *(const f32x4*)(cst + 32 * t + 8 * g + 4 * h), cu = *(const f32x4*)(cst + 32 * t + 16 + 8 * g + 4 * h);
#pragma unroll
      for (int i = 0; i < 4; i += 2) {
        const f32x2 ag = {acc[t][4 * g + i], acc[t][4 * g + i + 1]}, au = {acc[t][8 + 4 * g + i], acc[t][8 + 4 * g + i + 1]};
        const f32x2 gv = __builtin_elementwise_fma(f32x2{cg[i], cg[i + 1]}, nm, ag);
        const f32x2 uv = __builtin_elementwise_fma(f32x2{cu[i], cu[i + 1]}, nm, au) * rr2;
        const f32x2 m = f32x2{fmaxf(gv[0], 0.f), fmaxf(gv[1], 0.f)} * uv;
        v[4 * g + i] = m[0]; v[4 * g + i + 1] = m[1];
      }
    }
    st16_dev(rws, lane * 16, dst + t * 2048, as_u4(pack8(v[0], v[1], v[2], v[3], v[4], v[5], v[6], v[7])));
  }
  WAIT_VM(0); BARRIER();
  signal_panel(ws, 2, p);
}

#define XD5 8
constexpr int P5_SLOT = 32768, P5_XD = XD5;
DI constexpr int p5_slot(int it) { return ((it + 3) & 3) * P5_SLOT; }

DI int p5_rot(int it, int rot) { const int v = it + rot; return v >= 7 ? v - 7 : v; }

DI void down_compute(char* smem, int w, int lane, char* ws, rsrc_t rws, char* out, int tp, int c, int rot) {
  const int r = lane & 31, h = lane >> 5, tt = w & 1, kh = w >> 1;
  const int tb = 2 * tp + tt, ntile = 2 * c + kh;
  u32x4 rh[2];
#pragma unroll
  for (int s = 0; s < 2; ++s) rh[s] = ld16_dev(rws, lane * 16, WS_H + (tb * 32 + 2 * ntile + s) * 1024);
  Polls pl;
  wait_panel(ws, 2, tp >> 1, (tp & 1) * 8 + c, w, pl);
  BARRIER();
  u32x4 xf[56];
  unsigned mb[7];
#pragma unroll
  for (int it = 0; it < 7; ++it) mb[it] = WS_M + ((tp * 112 + 56 * kh + 8 * p5_rot(it, rot)) * 2 + tt) * 1024;
  auto ldx = [&](int g) { xf[g] = ld16_dev(rws, lane * 16, mb[g >> 3] + (g & 7) * 2048); };
#pragma unroll
  for (int g = 0; g < P5_XD; ++g) ldx(g);
  const unsigned lb = lds_addr(smem) + kh * 16384;
  unsigned a0[2], a1[2];
#pragma unroll
  for (int b = 0; b < 2; ++b) { a0[b] = opaque(lb + wtr_off(lane, 0) + b * 65536); a1[b] = opaque(lb + wtr_off(lane, 1) + b * 65536); }
  bf16x8 wf[3][2];
  auto rd = [&](int g) {
    const int o = p5_slot(g >> 3) + (g & 7) * 2048, b = o >> 16, oo = o & 65535;
    wf[g % 3][0] = tr_frag_a(a0[b] + oo, a0[b] + oo + 1024);
    wf[g % 3][1] = tr_frag_a(a1[b] + oo, a1[b] + oo + 1024);
  };
  f32x16 acc[2] = {};
  rd(0); rd(1);
  SCHED_FENCE();
#pragma unroll
  for (int g = 0; g < 56; ++g) {
    const int n = g + 2;
    acc[0] = MFMA32(wf[g % 3][0], as_frag(xf[g]), acc[0]); SCHED_FENCE();
    if (g == 0) retire(pl);
    if (n < 56) {
      if ((n & 7) == 0) { WAIT_LGKM0(); BARRIER(); }
      rd(n);
    }
    if (g + P5_XD < 56) ldx(g + P5_XD);
    SCHED_FENCE();
    acc[1] = MFMA32(wf[g % 3][1], as_frag(xf[g]), acc[1]); SCHED_FENCE();
  }
  f32x16 a = pair_reduce<2>(smem, w, lane, acc);
  unsigned pk[8];
#pragma unroll
  for (int s = 0; s < 2; ++s) {
#pragma unroll
    for (int i = 0; i < 4; ++i) { a[8 * s + 2 * i] += bf_lo(rh[s][i]); a[8 * s + 2 * i + 1] += bf_hi(rh[s][i]); }
    const u32x4 u = as_u4(pack8(a[8 * s], a[8 * s + 1], a[8 * s + 2], a[8 * s + 3], a[8 * s + 4], a[8 * s + 5], a[8 * s + 6], a[8 * s + 7]));
#pragma unroll
    for (int i = 0; i < 4; ++i) pk[4 * s + i] = u[i];
  }
  char* dst = out + (size_t)(32 * tb + r) * 1024 + (32 * ntile + 8 * h) * 2;
#pragma unroll
  for (int s = 0; s < 2; ++s) {
    auto q0 = __builtin_amdgcn_permlane32_swap(pk[4 * s], pk[4 * s + 2], false, false);
    auto q1 = __builtin_amdgcn_permlane32_swap(pk[4 * s + 1], pk[4 * s + 3], false, false);
    *(u32x4*)(dst + s * 32) = u32x4{q0[0], q1[0], q0[1], q1[1]};
  }
}

DI void loader_345(char* smem, int lw, int lane, char* ws, rsrc_t rws, const char* wd, int j, int c, int rot) {
  f32x4 cv[8];
  const int cl = lane < 56 ? lane : 55;
#pragma unroll
  for (int i = 0; i < 8; ++i) cv[i] = *(const f32x4*)(ws + WS_CS + ((size_t)(8 * lw + i) * 112 + 7 * j) * 128 + cl * 16);
  SCHED_FENCE();
  auto wgu = [&](int s) {
#pragma unroll
    for (int i = 0; i < 7; ++i) blds16(rws, lane * 16, WS_WGU + (j * 32 * 7 + s * 28 + lw * 7 + i) * 1024, smem + (s % 5) * P4_SLOT + (lw * 7 + i) * 1024);
  };
  const rsrc_t rwd = make_rsrc(wd, F * D * 2);
  const unsigned vo = wrow_voff(lane);
  auto wdn = [&](int it) {
#pragma unroll
    for (int i = 0; i < 8; ++i) {
      const int pc = lw * 8 + i, kh = pc >> 4, rr = (pc & 15) * 8;
      blds16(rwd, vo, (896 * kh + 128 * p5_rot(it, rot) + rr) * 1024 + c * 128, smem + p5_slot(it) + pc * 1024);
    }
  };
  wgu(0); wgu(1);
  BARRIER();
  BARRIER();
  wgu(2); wgu(3); wgu(4);
  {
    f32x4 a = cv[0];
#pragma unroll
    for (int i = 1; i < 8; ++i) a += cv[i];
    if (lane < 56) *(f32x4*)(smem + L_CS + lw * 896 + lane * 16) = a;
  }
  WAIT_LGKM0(); BARRIER();
  if (lw == 0 && lane < 56) {
    const char* t = smem + L_CS + lane * 16;
    *(f32x4*)(smem + L_CS + lane * 16) = *(const f32x4*)t + *(const f32x4*)(t + 896) + *(const f32x4*)(t + 1792) + *(const f32x4*)(t + 2688);
  }
  WAIT_VM(28); WAIT_LGKM0(); BARRIER();
  WAIT_VM(21); BARRIER(); wgu(5);
  WAIT_VM(21); BARRIER(); wgu(6);
  WAIT_VM(21); BARRIER(); wgu(7);
  WAIT_VM(21); BARRIER();
  WAIT_VM(14); BARRIER(); wdn(0);
  WAIT_VM(15); BARRIER();
  WAIT_VM(8); BARRIER(); wdn(1);
  BARRIER(); wdn(2); wdn(3);
  BARRIER();
  WAIT_VM(24); BARRIER();
  WAIT_VM(16); BARRIER(); wdn(4);
  WAIT_VM(16); BARRIER(); wdn(5);
  WAIT_VM(16); BARRIER(); wdn(6);
  WAIT_VM(16); BARRIER();
  WAIT_VM(8); BARRIER();
  WAIT_VM(0); BARRIER();
  BARRIER();
}

extern "C" __global__ __launch_bounds__(512, 2) void k_main(char* __restrict__ ws, const char* __restrict__ x, const char* __restrict__ wo,
                                                 const char* __restrict__ wd, char* __restrict__ out) {
  extern __shared__ __attribute__((aligned(16))) char smem[];
  const int w = __builtin_amdgcn_readfirstlane(threadIdx.x >> 6), lane = threadIdx.x & 63;
  const rsrc_t rws = make_rsrc(ws, WS_BYTES);
  phase_attn(smem, w, lane, ws, rws, wo);
  const int xcd = blockIdx.x & 7, i = blockIdx.x >> 3;
  const int tp = 4 * xcd + (i & 3), c = i >> 2;
  const int p4 = 8 * (xcd & 1) + (i & 7), j4 = 4 * (xcd >> 1) + (i >> 3);
  const int rot = (c + 2 * (i & 3)) % 7;
  if (w >= 4) loader_345(smem, w - 4, lane, ws, rws, wd, j4, c, rot);
  else {
    oproj_compute(smem, w, lane, ws, rws, x, tp, c);
    gateup_compute(smem, w, lane, ws, rws, p4, j4);
    down_compute(smem, w, lane, ws, rws, out, tp, c, rot);
  }
}

extern "C" void kernel_launch(void* const* d_in, const int* in_sizes, int n_in, void* d_out, int out_size, void* d_ws, size_t ws_size,
                              hipStream_t stream) {
  const char* x = (const char*)d_in[0];
  const char* wqkv = (const char*)d_in[1];
  const char* wo = (const char*)d_in[2];
  const char* wg = (const char*)d_in[4];
  const char* wu = (const char*)d_in[5];
  const char* wd = (const char*)d_in[6];
  char* ws = (char*)d_ws;
  (void)hipFuncSetAttribute((const void*)k_qkv, hipFuncAttributeMaxDynamicSharedMemorySize, P1_LDS);
  (void)hipFuncSetAttribute((const void*)k_main, hipFuncAttributeMaxDynamicSharedMemorySize, MAIN_LDS);
  k_qkv<<<256, 512, P1_LDS, stream>>>(x, wqkv, wg, wu, ws);
  k_main<<<256, 512, MAIN_LDS, stream>>>(ws, x, wo, wd, (char*)d_out);
}
```

```cpp
#include <hip/hip_runtime.h>
#include <hip/hip_bf16.h>
#include <stdint.h>

typedef __attribute__((ext_vector_type(8))) __bf16 bf16x8;
typedef __attribute__((ext_vector_type(2))) __bf16 bf16x2;
typedef __attribute__((ext_vector_type(16))) float f32x16;
typedef __attribute__((ext_vector_type(8))) float f32x8;
typedef __attribute__((ext_vector_type(4))) float f32x4;
typedef __attribute__((ext_vector_type(2))) float f32x2;
typedef __attribute__((ext_vector_type(4))) unsigned u32x4;
typedef __attribute__((ext_vector_type(2))) unsigned u32x2;
typedef __attribute__((ext_vector_type(4))) short s16x4;
typedef __attribute__((address_space(3))) s16x4* lds_s16x4;
typedef __attribute__((address_space(3))) void* lds_vp;
typedef const __attribute__((address_space(1))) void* g_vp;

#define DI __device__ __forceinline__
#define MFMA32(a, b, c) __builtin_amdgcn_mfma_f32_32x32x16_bf16((a), (b), (c), 0, 0, 0)
#define BARRIER() asm volatile("s_barrier" ::: "memory")
#define WAIT_VM(n) asm volatile("s_waitcnt vmcnt(" #n ")" ::: "memory")
#define WAIT_LGKM0() asm volatile("s_waitcnt lgkmcnt(0)" ::: "memory")
#define SCHED_FENCE() __builtin_amdgcn_sched_barrier(0)

constexpr int S = 2048, D = 512, F = 1792;
constexpr float LN_EPS = 1e-5f;
constexpr float Q_SCALE = 0.25f * 1.4426950408889634f;

constexpr unsigned WS_Q = 0;
constexpr unsigned WS_K = WS_Q + (2u << 20);
constexpr unsigned WS_V = WS_K + (2u << 20);
constexpr unsigned WS_O = WS_V + (2u << 20);
constexpr unsigned WS_H = WS_O + (2u << 20);
constexpr unsigned WS_ST = WS_H + (2u << 20);
constexpr unsigned WS_M = WS_ST + (256u << 10);
constexpr unsigned WS_WGU = WS_M + (7u << 20);
constexpr unsigned WS_CS = WS_WGU + (3584u << 10);
constexpr unsigned WS_CNT = WS_CS + (448u << 10);
constexpr unsigned WS_BYTES = WS_CNT + 768 * 128;

typedef __amdgpu_buffer_rsrc_t rsrc_t;
typedef __attribute__((address_space(3))) const bf16x8* lds_fp;
typedef __attribute__((address_space(1))) unsigned gu32;
constexpr int SC1 = 16;
constexpr unsigned QKV_CODE_BYTES = 36864, MAIN_CODE_BYTES = 25216;

DI rsrc_t make_rsrc(const void* p, unsigned bytes) { return __builtin_amdgcn_make_buffer_rsrc((void*)p, 0, bytes, 0x00020000); }
DI void glds16(const void* g, void* l) { __builtin_amdgcn_global_load_lds((g_vp)g, (lds_vp)l, 16, 0, 0); }
DI void blds16(rsrc_t rs, unsigned voff, unsigned soff, char* l) { __builtin_amdgcn_raw_ptr_buffer_load_lds(rs, (lds_vp)l, 16, voff, soff, 0, 0); }
DI u32x4 ld16_dev(rsrc_t rs, unsigned voff, unsigned soff) { return __builtin_amdgcn_raw_buffer_load_b128(rs, voff, soff, SC1); }
DI void st16_dev(rsrc_t rs, unsigned voff, unsigned soff, u32x4 v) { __builtin_amdgcn_raw_buffer_store_b128(v, rs, voff + soff, 0, SC1); }
constexpr int P1_AUXBITS = SC1;
DI void st16_p1(rsrc_t rs, unsigned voff, unsigned soff, u32x4 v) { __builtin_amdgcn_raw_buffer_store_b128(v, rs, voff + soff, 0, P1_AUXBITS); }
DI bf16x8 lds_frag(const char* p) { return *(const bf16x8*)p; }
DI bf16x8 lds_frag_a(unsigned a) { return *(lds_fp)(size_t)a; }
DI unsigned lds_addr(const char* p) { return (unsigned)(size_t)(const __attribute__((address_space(3))) char*)p; }
DI unsigned touch(const char* g) { unsigned v; asm volatile("global_load_dword %0, %1, off" : "=v"(v) : "v"(g)); return v; }
#define KERNEL_ADDR(dst, sym) do { unsigned lo_, hi_; \
  asm volatile("s_getpc_b64 s[40:41]\n\ts_add_u32 s40, s40, " #sym "@rel32@lo+4\n\ts_addc_u32 s41, s41, " #sym "@rel32@hi+12\n\ts_mov_b32 %0, s40\n\ts_mov_b32 %1, s41" \
               : "=s"(lo_), "=s"(hi_) :: "s40", "s41", "scc"); \
  dst = ((unsigned long long)hi_ << 32) | lo_; } while (0)
DI void dma_piece(u32x4 rs, unsigned voff, unsigned soff, unsigned lds) {
  asm volatile("s_mov_b32 m0, %0\n\ts_nop 0\n\tbuffer_load_dwordx4 %1, %2, %3 offen lds" :: "s"(lds), "v"(voff), "s"(rs), "s"(soff));
}
DI unsigned opaque(unsigned v) { asm volatile("" : "+v"(v)); return v; }
DI bf16x8 tr_frag(const char* p0, const char* p1) {
  s16x4 lo = __builtin_amdgcn_ds_read_tr16_b64_v4i16((lds_s16x4)p0);
  s16x4 hi = __builtin_amdgcn_ds_read_tr16_b64_v4i16((lds_s16x4)p1);
  return __builtin_bit_cast(bf16x8, __builtin_shufflevector(lo, hi, 0, 1, 2, 3, 4, 5, 6, 7));
}
DI bf16x8 tr_frag_a(unsigned a0, unsigned a1) {
  s16x4 lo = __builtin_amdgcn_ds_read_tr16_b64_v4i16((lds_s16x4)(size_t)a0);
  s16x4 hi = __builtin_amdgcn_ds_read_tr16_b64_v4i16((lds_s16x4)(size_t)a1);
  return __builtin_bit_cast(bf16x8, __builtin_shufflevector(lo, hi, 0, 1, 2, 3, 4, 5, 6, 7));
}
DI bf16x8 pack8(float a0, float a1, float a2, float a3, float a4, float a5, float a6, float a7) {
  f32x8 v = {a0, a1, a2, a3, a4, a5, a6, a7};
  return __builtin_convertvector(v, bf16x8);
}
DI u32x4 as_u4(bf16x8 v) { return __builtin_bit_cast(u32x4, v); }
DI bf16x8 as_frag(u32x4 v) { return __builtin_bit_cast(bf16x8, v); }
DI float bf_lo(unsigned u) { return __uint_as_float(u << 16); }
DI float bf_hi(unsigned u) { return __uint_as_float(u & 0xffff0000u); }
DI float half_swap_sum(float v) {
  auto r = __builtin_amdgcn_permlane32_swap(__float_as_uint(v), __float_as_uint(v), false, false);
  return __uint_as_float(r[0]) + __uint_as_float(r[1]);
}
DI float half_swap_max(float v) {
  auto r = __builtin_amdgcn_permlane32_swap(__float_as_uint(v), __float_as_uint(v), false, false);
  return fmaxf(__uint_as_float(r[0]), __uint_as_float(r[1]));
}
DI void add_f32(float& a, float v) { asm("v_add_f32 %0, %0, %1" : "+v"(a) : "v"(v)); }
DI void fma_sq(float& a, float v) { asm("v_fma_f32 %0, %1, %1, %0" : "+v"(a) : "v"(v)); }
DI float colsum_frag(bf16x8 f, float acc) {
  const bf16x2 ones = {(__bf16)1.0f, (__bf16)1.0f};
  const u32x4 u = as_u4(f);
#pragma unroll
  for (int i = 0; i < 4; ++i) { const unsigned e = u[i]; acc = __builtin_amdgcn_fdot2_f32_bf16(__builtin_bit_cast(bf16x2, e), ones, acc, false); }
  return acc;
}

DI void signal_panel(char* ws, int phase, int panel) {
  if (threadIdx.x < 16)
    __hip_atomic_fetch_add((gu32*)(ws + WS_CNT + ((phase * 16 + panel) * 16 + threadIdx.x) * 128), 1u, __ATOMIC_RELAXED, __HIP_MEMORY_SCOPE_AGENT);
}
struct Polls { unsigned v[4]; };
DI void wait_panel(char* ws, int phase, int panel, int replica, int w, Polls& p) {
  const char* word = ws + WS_CNT + ((phase * 16 + panel) * 16 + replica) * 128;
  unsigned left = 1u << 17, seen;
  unsigned zero = 0;
  asm volatile(
      "s_cmp_lg_u32 %8, 0\n\ts_cbranch_scc1 2f\n"
      "1:\n\tglobal_load_dword %0, %6, %7 sc1\n\ts_waitcnt vmcnt(0)\n\tv_readfirstlane_b32 %5, %0\n\ts_cmp_ge_u32 %5, 16\n\ts_cbranch_scc1 2f\n\t"
      "s_sub_u32 %4, %4, 1\n\ts_cmp_lg_u32 %4, 0\n\ts_cbranch_scc1 1b\n"
      "2:"
      : "=&v"(p.v[0]), "=&v"(p.v[1]), "=&v"(p.v[2]), "=&v"(p.v[3]), "+s"(left), "=&s"(seen)
      : "v"(zero), "s"(word), "s"(w)
      : "scc", "memory");
}
DI void retire(const Polls& p) { asm volatile("; retire %0 %1 %2 %3" :: "v"(p.v[0]), "v"(p.v[1]), "v"(p.v[2]), "v"(p.v[3])); }
constexpr int P1_SLOT = 28672, P1_NSLOT = 5, P1_AUX = P1_SLOT * P1_NSLOT, P1_PREP = P1_AUX + 4096;
constexpr int P1_LDS = P1_PREP + 16384;

template <int VMASK>
DI void p1_compute(char* smem, int w, int lane, int p, int j, const char* wqkv, char* ws) {
  const int r = lane & 31, h = lane >> 5;
  const int row = 32 * w + r, sw = (r >> 1) & 7;
  const unsigned lb = lds_addr(smem);
  unsigned xa[4][3], wa[3];
#pragma unroll
  for (int ks = 0; ks < 4; ++ks) {
    const unsigned o = lb + row * 128 + (((2 * ks + h) ^ sw) << 4);
#pragma unroll
    for (int b = 0; b < 3; ++b) xa[ks][b] = opaque(o + b * 2 * P1_SLOT);
  }
  {
    const int q = (lane & 15) >> 2, pp = lane & 3, blk = (lane >> 4) & 1;
    const unsigned o = lb + 16384 + (8 * h + q) * 192 + (2 * blk + (pp >> 1)) * 16 + 8 * (pp & 1);
#pragma unroll
    for (int b = 0; b < 3; ++b) wa[b] = opaque(o + b * 2 * P1_SLOT);
  }
  f32x16 acc[3] = {};
  float cs[3] = {0.f, 0.f, 0.f};
  bf16x8 xf[3], wf[3][3];
  auto rdx = [&](int g) { const int sl = (g >> 2) % P1_NSLOT; xf[g % 3] = lds_frag_a(xa[g & 3][sl >> 1] + (sl & 1) * P1_SLOT); };
  auto rdw = [&](int g, int c) {
    const int sl = (g >> 2) % P1_NSLOT;
    const unsigned a = wa[sl >> 1] + (sl & 1) * P1_SLOT + (16 * (g & 3)) * 192 + c * 64;
    wf[g % 3][c] = tr_frag_a(a, a + 4 * 192);
  };
  auto mm = [&](int g, int c) {
    if ((VMASK >> c) & 1) acc[c] = MFMA32(xf[g % 3], wf[g % 3][c], acc[c]);
    else acc[c] = MFMA32(wf[g % 3][c], xf[g % 3], acc[c]);
  };
  const unsigned long long wa64 = (unsigned long long)wqkv;
  const u32x4 rw = {(unsigned)wa64, (unsigned)(wa64 >> 32) & 0xffffu, (unsigned)(D * 3 * D * 2), 0x00020000u};
  unsigned wo_[3];
#pragma unroll
  for (int i = 0; i < 3; ++i) { const int g = 64 * (3 * w + i) + lane; wo_[i] = (g / 12) * 3072 + 192 * j + (g % 12) * 16; }
  auto wpiece = [&](int s, int i) { dma_piece(rw, wo_[i], s * 64 * 3072, lb + (s % P1_NSLOT) * P1_SLOT + 16384 + (3 * w + i) * 1024); };
#pragma unroll
  for (int s = 0; s < 3; ++s) { wpiece(s, 0); wpiece(s, 1); wpiece(s, 2); }
  unsigned warm;
  {
    unsigned long long ka, kb;
    KERNEL_ADDR(ka, k_qkv); KERNEL_ADDR(kb, k_main);
    const unsigned i = blockIdx.x >> 3, v = 4 * w + (lane & 3);
    const char* src = v < 9 ? (const char*)ka + min((i * 9 + v) * 128u, QKV_CODE_BYTES - 128u) : (const char*)kb + min((i * 7 + v - 9) * 128u, MAIN_CODE_BYTES - 128u);
    asm volatile("global_load_dword %0, %1, off" : "=v"(warm) : "v"(src));
  }
  WAIT_VM(7); BARRIER();
#pragma unroll
  for (int g = 0; g < 2; ++g) { rdx(g); rdw(g, 0); rdw(g, 1); rdw(g, 2); }
  SCHED_FENCE();
#pragma unroll
  for (int g = 0; g < 32; ++g) {
    const int n = g + 2;
    mm(g, 0); SCHED_FENCE();
    if (n < 32) {
      if ((n & 3) == 0) {
        if (n <= 8) WAIT_VM(7); else if (n <= 20) WAIT_VM(6); else if (n == 24) WAIT_VM(3); else WAIT_VM(0);
        WAIT_LGKM0(); BARRIER();
      }
      rdx(n); rdw(n, 0); SCHED_FENCE();
    }
    mm(g, 1); SCHED_FENCE();
    {
      const int m = g < 2 ? g : g + 2;
      const int s = g < 2 ? 3 : (m >> 2) + 3, i = g < 2 ? g : (m & 3);
      if (s < 8 && i < 3) { wpiece(s, i); SCHED_FENCE(); }
      if (g == 1) { wpiece(3, 2); SCHED_FENCE(); }
    }
    if (n < 32) { rdw(n, 1); rdw(n, 2); SCHED_FENCE(); }
    mm(g, 2); SCHED_FENCE();
    if ((g & 3) == w) {
#pragma unroll
      for (int c = 0; c < 3; ++c) cs[c] = colsum_frag(wf[g % 3][c], cs[c]);
    }
    SCHED_FENCE();
  }
  asm volatile("; warm %0" :: "v"(warm));
  float* csp = (float*)(smem + P1_AUX);
  float* cst = csp + 384;
  f32x2* st = (f32x2*)(smem + P1_AUX + 2048);
#pragma unroll
  for (int c = 0; c < 3; ++c) { float t = half_swap_sum(cs[c]); if (h == 0) csp[w * 96 + c * 32 + r] = t; }
  WAIT_LGKM0(); BARRIER();
  if (w < 2) { int t = w * 64 + lane; if (t < 96) cst[t] = csp[t] + csp[96 + t] + csp[192 + t] + csp[288 + t]; }
  WAIT_LGKM0(); BARRIER();
  const int tb = 4 * p + w;
  const rsrc_t rws = make_rsrc(ws, WS_BYTES);
  const f32x2 mine = st[row];
#pragma unroll
  for (int c = 0; c < 3; ++c) {
    const int c0 = 96 * j + 32 * c;
    if ((VMASK >> c) & 1) {
      const float csv = cst[32 * c + r];
      float v[16];
#pragma unroll
      for (int i = 0; i < 16; ++i) { f32x2 t = st[32 * w + (i & 3) + 8 * (i >> 2) + 4 * h]; v[i] = acc[c][i] * t[0] - t[1] * csv; }
      const unsigned dst = WS_V + (((c0 - 1024) >> 5) * 128 + 2 * tb) * 1024;
      st16_p1(rws, lane * 16, dst, as_u4(pack8(v[0], v[1], v[2], v[3], v[4], v[5], v[6], v[7])));
      st16_p1(rws, lane * 16, dst + 1024, as_u4(pack8(v[8], v[9], v[10], v[11], v[12], v[13], v[14], v[15])));
    } else {
      const bool isq = c0 < 512;
      const float sc = isq ? Q_SCALE : 1.0f;
      const float a = mine[0] * sc, b = mine[1] * sc;
      float v[16];
#pragma unroll
      for (int g = 0; g < 4; ++g) {
        f32x4 cv = *(const f32x4*)(cst + 32 * c + 8 * g + 4 * h);
#pragma unroll
        for (int i = 0; i < 4; ++i) v[4 * g + i] = acc[c][4 * g + i] * a - b * cv[i];
      }
      const int head = (c0 & 511) >> 4;
      const unsigned dst = (isq ? WS_Q : WS_K) + (head * 64 + tb) * 1024;
      st16_p1(rws, lane * 16, dst, as_u4(pack8(v[0], v[1], v[2], v[3], v[4], v[5], v[6], v[7])));
      st16_p1(rws, lane * 16, dst + 65536, as_u4(pack8(v[8], v[9], v[10], v[11], v[12], v[13], v[14], v[15])));
    }
  }
}

DI void p1_helper(char* smem, int lw, int lane, int p, int j, const char* x, const char* wg, const char* wu, char* ws) {
  const rsrc_t rx = make_rsrc(x, S * D * 2);
  unsigned xo[4];
#pragma unroll
  for (int i = 0; i < 4; ++i) {
    int row = 32 * lw + 8 * i + (lane >> 3);
    xo[i] = (128 * p + row) * 1024 + (((lane & 7) ^ ((row >> 1) & 7)) << 4);
  }
  auto issue = [&](int s) {
    char* sl = smem + (s % P1_NSLOT) * P1_SLOT;
#pragma unroll
    for (int i = 0; i < 4; ++i) blds16(rx, xo[i], s * 128, sl + (4 * lw + i) * 1024);
  };
  issue(0); issue(1); issue(2);
  const int b = blockIdx.x, cg = b % 28, pks = 4 * (b / 28) + lw;
  const bool prep = b < 224;
  char* stg = smem + P1_PREP + lw * 4096;
  auto prep_issue = [&]() {
    const rsrc_t rg = make_rsrc(wg, D * F * 2), ru = make_rsrc(wu, D * F * 2);
    const unsigned off = (16 * pks + (lane >> 3)) * 3584 + cg * 128 + (lane & 7) * 16;
    blds16(rg, off, 0, stg); blds16(rg, off, 8 * 3584, stg + 1024);
    blds16(ru, off, 0, stg + 2048); blds16(ru, off, 8 * 3584, stg + 3072);
  };
  const int r = lane & 31, h = lane >> 5, row = 32 * lw + r, sw = (r >> 1) & 7;
  bf16x8 pf_[4];
  auto prep_read = [&]() {
    const int blk = (lane >> 4) & 1, q = (lane & 15) >> 2, pp = lane & 3;
    const unsigned bs = lds_addr(stg) + blk * 2048 + (4 * h + q) * 128 + (pp >> 1) * 16 + 8 * (pp & 1);
    u32x2 t[8];
#define PREP_RD(i, off) asm volatile("ds_read_b64_tr_b16 %0, %1 offset:" #off : "=v"(t[i]) : "v"(bs))
    PREP_RD(0, 0); PREP_RD(1, 1024); PREP_RD(2, 32); PREP_RD(3, 1056); PREP_RD(4, 64); PREP_RD(5, 1088); PREP_RD(6, 96); PREP_RD(7, 1120);
#undef PREP_RD
    asm volatile("s_waitcnt lgkmcnt(0)" : "+v"(t[0]), "+v"(t[1]), "+v"(t[2]), "+v"(t[3]), "+v"(t[4]), "+v"(t[5]), "+v"(t[6]), "+v"(t[7]));
#pragma unroll
    for (int i = 0; i < 4; ++i) { const u32x2 lo = t[2 * i], hi = t[2 * i + 1]; pf_[i] = as_frag(u32x4{lo[0], lo[1], hi[0], hi[1]}); }
  };
  auto prep_frag = [&](int i) {
    const int fs = 4 * cg + i, jj = fs / 7, t = fs % 7;
    st16_p1(make_rsrc(ws, WS_BYTES), lane * 16, WS_WGU + ((jj * 32 + pks) * 7 + t) * 1024, as_u4(pf_[i]));
    const float c = half_swap_sum(colsum_frag(pf_[i], 0.f));
    if (h == 0) *(float*)(ws + WS_CS + ((size_t)pks * 112 + fs) * 128 + r * 4) = c;
  };
  float sum0 = 0.f, sum1 = 0.f, sq0 = 0.f, sq1 = 0.f;
  auto piece = [&](int s, int i) { blds16(rx, xo[i], s * 128, smem + (s % P1_NSLOT) * P1_SLOT + (4 * lw + i) * 1024); };
  auto step = [&](int s, int n) {
    const char* sl = smem + (s % P1_NSLOT) * P1_SLOT + row * 128;
    u32x4 u[4];
#pragma unroll
    for (int c = 0; c < 4; ++c) u[c] = *(const u32x4*)(sl + (((4 * h + c) ^ sw) << 4));
    SCHED_FENCE();
#pragma unroll
    for (int c = 0; c < 4; ++c) {
      if (n < 8) piece(n, c);
      SCHED_FENCE();
#pragma unroll
      for (int i = 0; i < 4; ++i) {
        const float lo = bf_lo(u[c][i]), hi = bf_hi(u[c][i]);
        add_f32(sum0, lo); add_f32(sum1, hi); fma_sq(sq0, lo); fma_sq(sq1, hi);
      }
      SCHED_FENCE();
    }
  };
  if (prep) {
    prep_issue();
    WAIT_VM(12); BARRIER();
    step(0, 3); WAIT_VM(12); WAIT_LGKM0(); BARRIER();
    step(1, 4); WAIT_VM(12); WAIT_LGKM0(); BARRIER();
  } else {
    WAIT_VM(8); BARRIER();
    step(0, 3); WAIT_VM(8); WAIT_LGKM0(); BARRIER();
    step(1, 4); WAIT_VM(8); WAIT_LGKM0(); BARRIER();
  }
  step(2, 5); WAIT_VM(8); WAIT_LGKM0(); BARRIER();
  step(3, 6); WAIT_VM(8); WAIT_LGKM0(); BARRIER();
  step(4, 7); WAIT_VM(8); WAIT_LGKM0(); BARRIER();
  step(5, 8); WAIT_VM(4);
  if (prep) prep_read();
  WAIT_LGKM0(); BARRIER();
  step(6, 8); WAIT_VM(0);
  if (prep) prep_frag(0);
  WAIT_LGKM0(); BARRIER();
  step(7, 8);
  const float s1 = half_swap_sum(sum0 + sum1), s2 = half_swap_sum(sq0 + sq1);
  const float mean = s1 * (1.0f / D);
  const float var = fmaxf(s2 * (1.0f / D) - mean * mean, 0.f);
  const float rstd = 1.0f / sqrtf(var + LN_EPS);
  f32x2* st = (f32x2*)(smem + P1_AUX + 2048);
  if (h == 0) st[row] = f32x2{rstd, mean * rstd};
  WAIT_LGKM0(); BARRIER();
  BARRIER();
  if (prep) prep_frag(1);
  if (prep) { prep_frag(2); prep_frag(3); }
}

extern "C" __global__ __launch_bounds__(512, 2) void k_qkv(const char* __restrict__ x, const char* __restrict__ wqkv, const char* __restrict__ wg,
                                                const char* __restrict__ wu, char* __restrict__ ws) {
  extern __shared__ __attribute__((aligned(16))) char smem[];
  const int w = __builtin_amdgcn_readfirstlane(threadIdx.x >> 6), lane = threadIdx.x & 63;
  const int xcd = blockIdx.x & 7, i = blockIdx.x >> 3;
  const int p = 4 * (xcd & 3) + (i & 3), j = 8 * (xcd >> 2) + (i >> 2);
  if (threadIdx.x < 3) *(unsigned*)(ws + WS_CNT + (blockIdx.x * 3 + threadIdx.x) * 128) = 0u;
  if (w >= 4) p1_helper(smem, w - 4, lane, p, j, x, wg, wu, ws);
  else if (j <= 9) p1_compute<0>(smem, w, lane, p, j, wqkv, ws);
  else if (j == 10) p1_compute<4>(smem, w, lane, p, j, wqkv, ws);
  else p1_compute<7>(smem, w, lane, p, j, wqkv, ws);
}

constexpr int P2_K = 0, P2_V = 24576, P2_VP = 1040, P2_Q = P2_V + 24 * P2_VP;
constexpr int L_WO = 77824;
constexpr int L_RED = 143360;
constexpr int L_CS = L_RED + 16384;
constexpr int MAIN_LDS = 163840;

template <bool FULL>
DI void attn_task(const char* smem, int lane, int hh, int wq, int qb, rsrc_t rws, int head) {
  const int r = lane & 31, h = lane >> 5;
  const char* kb_ = smem + P2_K + (hh * 12 + wq) * 1024 + lane * 16;
  const bool valid = (r >> 4) == hh;
  const char* vb_ = smem + P2_V + 2 * wq * P2_VP + (valid ? lane * 16 : 1024);
  const bf16x8 qf = lds_frag(smem + P2_Q + (hh * 4 + wq) * 1024 + lane * 16);
  const f32x16 zero = {};
  const int t0 = FULL ? 1 : ((8 - qb) > 1 ? (8 - qb) : 1);
  const bool have_first = FULL || qb >= 8;
  f32x16 sm;
  {
    const f32x16 sd = MFMA32(lds_frag(kb_ + 8 * 1024), qf, zero);
    f32x16 sf;
    if (have_first) sf = MFMA32(lds_frag(kb_), qf, zero);
    else { for (int i = 0; i < 16; ++i) sf[i] = -1e30f; }
#pragma unroll
    for (int i = 0; i < 16; ++i) { const int jj = (i & 3) + 8 * (i >> 2) + 4 * h; sm[i] = jj <= r ? sd[i] : sf[i]; }
  }
  float m = sm[0];
#pragma unroll
  for (int i = 1; i < 16; ++i) m = fmaxf(m, sm[i]);
  if (FULL) {
#pragma unroll
    for (int t = 1; t < 8; ++t) {
      const f32x16 s = MFMA32(lds_frag(kb_ + t * 1024), qf, zero);
#pragma unroll
      for (int i = 0; i < 16; i += 2) m = fmaxf(fmaxf(m, s[i]), s[i + 1]);
    }
  } else {
    for (int t = t0; t < 8; ++t) {
      const f32x16 s = MFMA32(lds_frag(kb_ + t * 1024), qf, zero);
#pragma unroll
      for (int i = 0; i < 16; i += 2) m = fmaxf(fmaxf(m, s[i]), s[i + 1]);
    }
  }
  m = half_swap_max(m);
  f32x16 negm;
#pragma unroll
  for (int i = 0; i < 16; ++i) negm[i] = -m;
  f32x16 o = zero;
  {
    float pv[16];
#pragma unroll
    for (int i = 0; i < 16; ++i) pv[i] = __builtin_amdgcn_exp2f(sm[i] - m);
    u32x4 pa = __builtin_bit_cast(u32x4, pack8(pv[0], pv[1], pv[2], pv[3], pv[4], pv[5], pv[6], pv[7]));
    u32x4 pb = __builtin_bit_cast(u32x4, pack8(pv[8], pv[9], pv[10], pv[11], pv[12], pv[13], pv[14], pv[15]));
    u32x4 da, db;
#pragma unroll
    for (int i = 0; i < 4; ++i) {
      const int ja = ((2 * i) & 3) + 8 * ((2 * i) >> 2) + 4 * h;
      const unsigned ma = (ja <= r ? 0xffffu : 0u) | (ja + 1 <= r ? 0xffff0000u : 0u);
      const unsigned mb = (ja + 16 <= r ? 0xffffu : 0u) | (ja + 17 <= r ? 0xffff0000u : 0u);
      da[i] = pa[i] & ma; db[i] = pb[i] & mb;
    }
    o = MFMA32(lds_frag(vb_ + 16 * P2_VP), __builtin_bit_cast(bf16x8, da), o);
    o = MFMA32(lds_frag(vb_ + 17 * P2_VP), __builtin_bit_cast(bf16x8, db), o);
    if (have_first) {
      o = MFMA32(lds_frag(vb_), __builtin_bit_cast(bf16x8, pa ^ da), o);
      o = MFMA32(lds_frag(vb_ + P2_VP), __builtin_bit_cast(bf16x8, pb ^ db), o);
    }
  }
  auto tile = [&](int t) {
    const f32x16 s = MFMA32(lds_frag(kb_ + t * 1024), qf, negm);
    float pv[16];
#pragma unroll
    for (int i = 0; i < 16; ++i) pv[i] = __builtin_amdgcn_exp2f(s[i]);
    o = MFMA32(lds_frag(vb_ + (2 * t) * P2_VP), pack8(pv[0], pv[1], pv[2], pv[3], pv[4], pv[5], pv[6], pv[7]), o);
    o = MFMA32(lds_frag(vb_ + (2 * t + 1) * P2_VP), pack8(pv[8], pv[9], pv[10], pv[11], pv[12], pv[13], pv[14], pv[15]), o);
  };
  if (FULL) {
#pragma unroll
    for (int t = 1; t < 8; ++t) tile(t);
  } else {
    for (int t = t0; t < 8; ++t) tile(t);
  }
  const float l = hh ? o[0] : o[8];
  const float inv = 1.0f / l;
  float ov[8];
#pragma unroll
  for (int i = 0; i < 8; ++i) ov[i] = (hh ? o[8 + i] : o[i]) * inv;
  st16_dev(rws, lane * 16, WS_O + (((qb >> 1) * 32 + head) * 2 + (qb & 1)) * 1024, as_u4(pack8(ov[0], ov[1], ov[2], ov[3], ov[4], ov[5], ov[6], ov[7])));
}

DI unsigned wrow_voff(int lane) { return (lane >> 3) * 1024 + (((lane & 7) ^ (((lane >> 4) & 1) << 2)) << 4); }
DI unsigned wtr_off(int lane, int nt) {
  const int h = lane >> 5, blk = (lane >> 4) & 1, q = (lane & 15) >> 2, pp = lane & 3, sw = (q >> 1) & 1;
  return (4 * h + q) * 128 + (2 * blk + (pp >> 1) + 4 * (nt ^ sw)) * 16 + 8 * (pp & 1);
}

DI void phase_attn(char* smem, int w, int lane, char* ws, rsrc_t rws, const char* wo) {
  const int xcd = blockIdx.x & 7, i = blockIdx.x >> 3;
  const int hp = 2 * xcd + (i & 1), p = i >> 1;
  const int kb0 = 4 * p - 8;
  const int hh = w >> 2, wq = w & 3, qb = 4 * p + wq, head = 2 * hp + hh;
  blds16(rws, lane * 16, WS_Q + (head * 64 + qb) * 1024, smem + P2_Q + w * 1024);
#pragma unroll
  for (int n = 0; n < 3; ++n) {
    const int t = wq * 3 + n, sv = w * 3 + n;
    const int kt = kb0 + t < 0 ? 0 : kb0 + t, vt = 2 * kb0 + sv < 0 ? 0 : 2 * kb0 + sv;
    blds16(rws, lane * 16, WS_K + (head * 64 + kt) * 1024, smem + P2_K + (hh * 12 + t) * 1024);
    blds16(rws, lane * 16, WS_V + (hp * 128 + vt) * 1024, smem + P2_V + sv * P2_VP);
  }
  if (threadIdx.x < 24) *(u32x4*)(smem + P2_V + threadIdx.x * P2_VP + 1024) = u32x4{0x3f803f80u, 0x3f803f80u, 0x3f803f80u, 0x3f803f80u};
  WAIT_VM(0); WAIT_LGKM0(); BARRIER();
  {
    const rsrc_t rwo = make_rsrc(wo, D * D * 2);
    const int c = i >> 2;
    const unsigned vo = wrow_voff(lane);
#pragma unroll
    for (int n = 0; n < 8; ++n) blds16(rwo, vo, (8 * w + n) * 8192 + c * 128, smem + L_WO + (8 * w + n) * 1024);
  }
  if (qb >= 8) attn_task<true>(smem, lane, hh, wq, qb, rws, head);
  else attn_task<false>(smem, lane, hh, wq, qb, rws, head);
  WAIT_VM(0); WAIT_LGKM0(); BARRIER();
  signal_panel(ws, 0, p);
}

template <int NACC>
DI f32x16 pair_reduce(char* smem, int w, int lane, const f32x16 (&acc)[NACC]) {
  char* dst = smem + L_RED + w * 4096 + lane * 16;
  auto put = [&](const f32x16& send) {
#pragma unroll
    for (int g = 0; g < 4; ++g) *(f32x4*)(dst + g * 1024) = f32x4{send[4 * g], send[4 * g + 1], send[4 * g + 2], send[4 * g + 3]};
  };
  f32x16 a;
  if (w >> 1) { put(acc[0]); a = acc[1]; asm volatile("" : "+v"(a)); }
  else { put(acc[1]); a = acc[0]; asm volatile("" : "+v"(a)); }
  WAIT_LGKM0(); BARRIER();
#pragma unroll
  for (int g = 0; g < 4; ++g) {
    const f32x4 v = *(const f32x4*)(smem + L_RED + ((w ^ 2) * 4 + g) * 1024 + lane * 16);
    a[4 * g] += v[0]; a[4 * g + 1] += v[1]; a[4 * g + 2] += v[2]; a[4 * g + 3] += v[3];
  }
  return a;
}

DI void oproj_compute(char* smem, int w, int lane, char* ws, rsrc_t rws, const char* x, int tp, int c) {
  const int r = lane & 31, h = lane >> 5, tt = w & 1, kh = w >> 1;
  const int tb = 2 * tp + tt, ntile = 2 * c + kh;
  u32x2 rx[4];
#pragma unroll
  for (int g = 0; g < 4; ++g) rx[g] = *(const u32x2*)(x + (size_t)(32 * tb + r) * 1024 + (32 * ntile + 8 * g + 4 * h) * 2);
  Polls pl;
  wait_panel(ws, 0, tp >> 1, (tp & 1) * 8 + c, w, pl);
  BARRIER();
  u32x4 xf[16];
#pragma unroll
  for (int g = 0; g < 16; ++g) xf[g] = ld16_dev(rws, lane * 16, WS_O + ((tp * 32 + 16 * kh + g) * 2 + tt) * 1024);
  const unsigned lb = lds_addr(smem) + L_WO + kh * 32768;
  const unsigned a0 = opaque(lb + wtr_off(lane, 0)), a1 = opaque(lb + wtr_off(lane, 1));
  bf16x8 wf[3][2];
  auto rd = [&](int g) {
    wf[g % 3][0] = tr_frag_a(a0 + g * 2048, a0 + g * 2048 + 1024);
    wf[g % 3][1] = tr_frag_a(a1 + g * 2048, a1 + g * 2048 + 1024);
  };
  f32x16 acc[2] = {};
  rd(0); rd(1);
  SCHED_FENCE();
#pragma unroll
  for (int g = 0; g < 16; ++g) {
    acc[0] = MFMA32(wf[g % 3][0], as_frag(xf[g]), acc[0]); SCHED_FENCE();
    if (g == 0) retire(pl);
    if (g + 2 < 16) { rd(g + 2); SCHED_FENCE(); }
    acc[1] = MFMA32(wf[g % 3][1], as_frag(xf[g]), acc[1]); SCHED_FENCE();
  }
  f32x16 a = pair_reduce<2>(smem, w, lane, acc);
#pragma unroll
  for (int g = 0; g < 4; ++g) {
    a[4 * g] += bf_lo(rx[g][0]); a[4 * g + 1] += bf_hi(rx[g][0]); a[4 * g + 2] += bf_lo(rx[g][1]); a[4 * g + 3] += bf_hi(rx[g][1]);
  }
  float s1 = 0.f, s2 = 0.f;
#pragma unroll
  for (int s = 0; s < 2; ++s) {
    const u32x4 u = as_u4(pack8(a[8 * s], a[8 * s + 1], a[8 * s + 2], a[8 * s + 3], a[8 * s + 4], a[8 * s + 5], a[8 * s + 6], a[8 * s + 7]));
    st16_dev(rws, lane * 16, WS_H + (tb * 32 + 2 * ntile + s) * 1024, u);
#pragma unroll
    for (int i = 0; i < 4; ++i) { const float lo = bf_lo(u[i]), hi = bf_hi(u[i]); s1 += lo + hi; s2 += lo * lo + hi * hi; }
  }
  s1 = half_swap_sum(s1); s2 = half_swap_sum(s2);
  if (h == 0) __builtin_amdgcn_raw_buffer_store_b64(u32x2{__float_as_uint(s1), __float_as_uint(s2)}, rws, r * 8, WS_ST + (tb * 16 + ntile) * 256, SC1);
  WAIT_VM(0); BARRIER();
  signal_panel(ws, 1, tp >> 1);
}

#define XD4 12
constexpr int P4_SLOT = 28672, P4_XD = XD4;

DI void gateup_compute(char* smem, int w, int lane, char* ws, rsrc_t rws, int p, int j) {
  const int r = lane & 31, h = lane >> 5, tb = 4 * p + w;
  Polls pl;
  wait_panel(ws, 1, p, j, w, pl);
  BARRIER();
  u32x4 xf[32];
  auto ldx = [&](int g) { xf[g] = ld16_dev(rws, lane * 16, WS_H + (tb * 32 + g) * 1024); };
  ldx(0); ldx(1);
  u32x2 sv[8];
#pragma unroll
  for (int i = 0; i < 8; ++i) sv[i] = __builtin_amdgcn_raw_buffer_load_b64(rws, (8 * h + i) * 256 + r * 8, WS_ST + tb * 4096, SC1);
#pragma unroll
  for (int g = 2; g < P4_XD; ++g) ldx(g);
  const unsigned lb = lds_addr(smem) + lane * 16;
  unsigned ba[3];
#pragma unroll
  for (int b = 0; b < 3; ++b) ba[b] = opaque(lb + b * 2 * P4_SLOT);
  bf16x8 wf[2][7];
  auto rdw = [&](int g, int t) {
    const int sl = (g >> 2) % 5;
    wf[g & 1][t] = lds_frag_a(ba[sl >> 1] + (sl & 1) * P4_SLOT + ((g & 3) * 7 + t) * 1024);
  };
  f32x16 acc[7] = {};
#pragma unroll
  for (int t = 0; t < 7; ++t) rdw(0, t);
  float s1 = 0.f, s2 = 0.f;
  SCHED_FENCE();
#pragma unroll
  for (int g = 0; g < 32; ++g) {
    const int n = g + 1;
#pragma unroll
    for (int t = 0; t < 7; ++t) {
      acc[t] = MFMA32(wf[g & 1][t], as_frag(xf[g]), acc[t]); SCHED_FENCE();
      if (g == 0 && t == 0) retire(pl);
      if (t == 0 && n < 32 && (n & 3) == 0) { WAIT_LGKM0(); BARRIER(); }
      if (t == 0 && n == 32) { WAIT_LGKM0(); BARRIER(); }
      if (n < 32) rdw(n, t);
      if (t == 1 && g + P4_XD < 32) ldx(g + P4_XD);
      if (g == 2 && t == 2) {
#pragma unroll
        for (int i = 0; i < 8; ++i) { const unsigned e0 = sv[i][0], e1 = sv[i][1]; s1 += __uint_as_float(e0); s2 += __uint_as_float(e1); }
      }
      SCHED_FENCE();
    }
  }
  s1 = half_swap_sum(s1); s2 = half_swap_sum(s2);
  const float mean = s1 * (1.0f / D);
  const float rstd = 1.0f / sqrtf(fmaxf(s2 * (1.0f / D) - mean * mean, 0.f) + LN_EPS);
  const float rr = rstd * rstd;
  const f32x2 nm = {-mean, -mean}, rr2 = {rr, rr};
  const float* cst = (const float*)(smem + L_CS);
  const unsigned dst = WS_M + (((tb >> 1) * 112 + 7 * j) * 2 + (tb & 1)) * 1024;
#pragma unroll
  for (int t = 0; t < 7; ++t) {
    float v[8];
#pragma unroll
    for (int g = 0; g < 2; ++g) {
      const f32x4 cg = *(const f32x4*)(cst + 32 * t + 8 * g + 4 * h), cu = *(const f32x4*)(cst + 32 * t + 16 + 8 * g + 4 * h);
#pragma unroll
      for (int i = 0; i < 4; i += 2) {
        const f32x2 ag = {acc[t][4 * g + i], acc[t][4 * g + i + 1]}, au = {acc[t][8 + 4 * g + i], acc[t][8 + 4 * g + i + 1]};
        const f32x2 gv = __builtin_elementwise_fma(f32x2{cg[i], cg[i + 1]}, nm, ag);
        const f32x2 uv = __builtin_elementwise_fma(f32x2{cu[i], cu[i + 1]}, nm, au) * rr2;
        const f32x2 m = f32x2{fmaxf(gv[0], 0.f), fmaxf(gv[1], 0.f)} * uv;
        v[4 * g + i] = m[0]; v[4 * g + i + 1] = m[1];
      }
    }
    st16_dev(rws, lane * 16, dst + t * 2048, as_u4(pack8(v[0], v[1], v[2], v[3], v[4], v[5], v[6], v[7])));
  }
  WAIT_VM(0); BARRIER();
  signal_panel(ws, 2, p);
}

#define XD5 8
constexpr int P5_SLOT = 32768, P5_XD = XD5;
DI constexpr int p5_slot(int it) { return ((it + 3) & 3) * P5_SLOT; }

DI int p5_rot(int it, int rot) { const int v = it + rot; return v >= 7 ? v - 7 : v; }

DI void down_compute(char* smem, int w, int lane, char* ws, rsrc_t rws, char* out, int tp, int c, int rot) {
  const int r = lane & 31, h = lane >> 5, tt = w & 1, kh = w >> 1;
  const int tb = 2 * tp + tt, ntile = 2 * c + kh;
  u32x4 rh[2];
#pragma unroll
  for (int s = 0; s < 2; ++s) rh[s] = ld16_dev(rws, lane * 16, WS_H + (tb * 32 + 2 * ntile + s) * 1024);
  Polls pl;
  wait_panel(ws, 2, tp >> 1, (tp & 1) * 8 + c, w, pl);
  BARRIER();
  u32x4 xf[56];
  unsigned mb[7];
#pragma unroll
  for (int it = 0; it < 7; ++it) mb[it] = WS_M + ((tp * 112 + 56 * kh + 8 * p5_rot(it, rot)) * 2 + tt) * 1024;
  auto ldx = [&](int g) { xf[g] = ld16_dev(rws, lane * 16, mb[g >> 3] + (g & 7) * 2048); };
#pragma unroll
  for (int g = 0; g < P5_XD; ++g) ldx(g);
  const unsigned lb = lds_addr(smem) + kh * 16384;
  unsigned a0[2], a1[2];
#pragma unroll
  for (int b = 0; b < 2; ++b) { a0[b] = opaque(lb + wtr_off(lane, 0) + b * 65536); a1[b] = opaque(lb + wtr_off(lane, 1) + b * 65536); }
  bf16x8 wf[3][2];
  auto rd = [&](int g) {
    const int o = p5_slot(g >> 3) + (g & 7) * 2048, b = o >> 16, oo = o & 65535;
    wf[g % 3][0] = tr_frag_a(a0[b] + oo, a0[b] + oo + 1024);
    wf[g % 3][1] = tr_frag_a(a1[b] + oo, a1[b] + oo + 1024);
  };
  f32x16 acc[2] = {};
  rd(0); rd(1);
  SCHED_FENCE();
#pragma unroll
  for (int g = 0; g < 56; ++g) {
    const int n = g + 2;
    acc[0] = MFMA32(wf[g % 3][0], as_frag(xf[g]), acc[0]); SCHED_FENCE();
    if (g == 0) retire(pl);
    if (n < 56) {
      if ((n & 7) == 0) { WAIT_LGKM0(); BARRIER(); }
      rd(n);
    }
    if (g + P5_XD < 56) ldx(g + P5_XD);
    SCHED_FENCE();
    acc[1] = MFMA32(wf[g % 3][1], as_frag(xf[g]), acc[1]); SCHED_FENCE();
  }
  f32x16 a = pair_reduce<2>(smem, w, lane, acc);
  unsigned pk[8];
#pragma unroll
  for (int s = 0; s < 2; ++s) {
#pragma unroll
    for (int i = 0; i < 4; ++i) { a[8 * s + 2 * i] += bf_lo(rh[s][i]); a[8 * s + 2 * i + 1] += bf_hi(rh[s][i]); }
    const u32x4 u = as_u4(pack8(a[8 * s], a[8 * s + 1], a[8 * s + 2], a[8 * s + 3], a[8 * s + 4], a[8 * s + 5], a[8 * s + 6], a[8 * s + 7]));
#pragma unroll
    for (int i = 0; i < 4; ++i) pk[4 * s + i] = u[i];
  }
  char* dst = out + (size_t)(32 * tb + r) * 1024 + (32 * ntile + 8 * h) * 2;
#pragma unroll
  for (int s = 0; s < 2; ++s) {
    auto q0 = __builtin_amdgcn_permlane32_swap(pk[4 * s], pk[4 * s + 2], false, false);
    auto q1 = __builtin_amdgcn_permlane32_swap(pk[4 * s + 1], pk[4 * s + 3], false, false);
    *(u32x4*)(dst + s * 32) = u32x4{q0[0], q1[0], q0[1], q1[1]};
  }
}

DI void loader_345(char* smem, int lw, int lane, char* ws, rsrc_t rws, const char* wd, int j, int c, int rot) {
  f32x4 cv[8];
  const int cl = lane < 56 ? lane : 55;
#pragma unroll
  for (int i = 0; i < 8; ++i) cv[i] = *(const f32x4*)(ws + WS_CS + ((size_t)(8 * lw + i) * 112 + 7 * j) * 128 + cl * 16);
  SCHED_FENCE();
  auto wgu = [&](int s) {
#pragma unroll
    for (int i = 0; i < 7; ++i) blds16(rws, lane * 16, WS_WGU + (j * 32 * 7 + s * 28 + lw * 7 + i) * 1024, smem + (s % 5) * P4_SLOT + (lw * 7 + i) * 1024);
  };
  const rsrc_t rwd = make_rsrc(wd, F * D * 2);
  const unsigned vo = wrow_voff(lane);
  auto wdn = [&](int it) {
#pragma unroll
    for (int i = 0; i < 8; ++i) {
      const int pc = lw * 8 + i, kh = pc >> 4, rr = (pc & 15) * 8;
      blds16(rwd, vo, (896 * kh + 128 * p5_rot(it, rot) + rr) * 1024 + c * 128, smem + p5_slot(it) + pc * 1024);
    }
  };
  wgu(0); wgu(1);
  BARRIER();
  BARRIER();
  wgu(2); wgu(3); wgu(4);
  {
    f32x4 a = cv[0];
#pragma unroll
    for (int i = 1; i < 8; ++i) a += cv[i];
    if (lane < 56) *(f32x4*)(smem + L_CS + lw * 896 + lane * 16) = a;
  }
  WAIT_LGKM0(); BARRIER();
  if (lw == 0 && lane < 56) {
    const char* t = smem + L_CS + lane * 16;
    *(f32x4*)(smem + L_CS + lane * 16) = *(const f32x4*)t + *(const f32x4*)(t + 896) + *(const f32x4*)(t + 1792) + *(const f32x4*)(t + 2688);
  }
  WAIT_VM(28); WAIT_LGKM0(); BARRIER();
  WAIT_VM(21); BARRIER(); wgu(5);
  WAIT_VM(21); BARRIER(); wgu(6);
  WAIT_VM(21); BARRIER(); wgu(7);
  WAIT_VM(21); BARRIER();
  WAIT_VM(14); BARRIER(); wdn(0);
  WAIT_VM(15); BARRIER();
  WAIT_VM(8); BARRIER(); wdn(1);
  BARRIER(); wdn(2); wdn(3);
  BARRIER();
  WAIT_VM(24); BARRIER();
  WAIT_VM(16); BARRIER(); wdn(4);
  WAIT_VM(16); BARRIER(); wdn(5);
  WAIT_VM(16); BARRIER(); wdn(6);
  WAIT_VM(16); BARRIER();
  WAIT_VM(8); BARRIER();
  WAIT_VM(0); BARRIER();
  BARRIER();
}

extern "C" __global__ __launch_bounds__(512, 2) void k_main(char* __restrict__ ws, const char* __restrict__ x, const char* __restrict__ wo,
                                                 const char* __restrict__ wd, char* __restrict__ out) {
  extern __shared__ __attribute__((aligned(16))) char smem[];
  const int w = __builtin_amdgcn_readfirstlane(threadIdx.x >> 6), lane = threadIdx.x & 63;
  const rsrc_t rws = make_rsrc(ws, WS_BYTES);
  phase_attn(smem, w, lane, ws, rws, wo);
  const int xcd = blockIdx.x & 7, i = blockIdx.x >> 3;
  const int tp = 4 * xcd + (i & 3), c = i >> 2;
  const int p4 = 8 * (xcd & 1) + (i & 7), j4 = 4 * (xcd >> 1) + (i >> 3);
  const int rot = (c + 2 * (i & 3)) % 7;
  if (w >= 4) loader_345(smem, w - 4, lane, ws, rws, wd, j4, c, rot);
  else {
    oproj_compute(smem, w, lane, ws, rws, x, tp, c);
    gateup_compute(smem, w, lane, ws, rws, p4, j4);
    down_compute(smem, w, lane, ws, rws, out, tp, c, rot);
  }
}

extern "C" void kernel_launch(void* const* d_in, const int* in_sizes, int n_in, void* d_out, int out_size, void* d_ws, size_t ws_size,
                              hipStream_t stream) {
  const char* x = (const char*)d_in[0];
  const char* wqkv = (const char*)d_in[1];
  const char* wo = (const char*)d_in[2];
  const char* wg = (const char*)d_in[4];
  const char* wu = (const char*)d_in[5];
  const char* wd = (const char*)d_in[6];
  char* ws = (char*)d_ws;
  (void)hipFuncSetAttribute((const void*)k_qkv, hipFuncAttributeMaxDynamicSharedMemorySize, P1_LDS);
  (void)hipFuncSetAttribute((const void*)k_main, hipFuncAttributeMaxDynamicSharedMemorySize, MAIN_LDS);
  k_qkv<<<256, 512, P1_LDS, stream>>>(x, wqkv, wg, wu, ws);
  k_main<<<256, 512, MAIN_LDS, stream>>>(ws, x, wo, wd, (char*)d_out);
}
```
